```python
import torch

def _softmax(x, axis=-1):
    return torch.softmax(x, dim=axis)
def _gelu(x):
    return torch.nn.functional.gelu(x, approximate="tanh")
def _silu(x):
    return torch.nn.functional.silu(x)
def _sigmoid(x):
    return torch.sigmoid(x)
def _layernorm(x, axis=-1, eps=1e-5):
    m = x.mean(dim=axis, keepdim=True)
    v = x.var(dim=axis, keepdim=True, unbiased=False)
    return (x - m) / torch.sqrt(v + eps)
def _scatter_add(tgt, idx, src, axis=0):
    return tgt.clone().index_add_(axis, idx, src)
def _segment_sum(src, seg, n):
    out = torch.zeros((n,) + src.shape[1:], dtype=src.dtype)
    return out.index_add_(0, seg, src)
def _pad(x, pw):
    flat = tuple(v for pair in reversed(pw) for v in pair)
    return torch.nn.functional.pad(x, flat)

kernel_name = 'sequential_preLN__sliding_block_cached__dense_activation_silu__decode'

def setup_inputs():

    _w = (lambda a: (lambda t: t.to(torch.bfloat16) if t.is_floating_point() else t)(torch.as_tensor(a)))
    def _A_setup():
        B, Sq, D = 4, 1, 512
        Hq, Hkv, Sk, Dh = 32, 8, 16384, 128
        assert Hq % Hkv == 0, (Hq, Hkv)
        return {
            "x": _w((lambda *s: torch.randn(*s, dtype=torch.bfloat16) * 0.05)(B, Sq, D)),
            "k_cache": _w((lambda *s: torch.randn(*s, dtype=torch.bfloat16) * 3.2)(B, Hkv, Sk, Dh)),
            "v_cache": _w((lambda *s: torch.randn(*s, dtype=torch.bfloat16) * 0.8)(B, Hkv, Sk, Dh)),
            "wq": _w((lambda *s: torch.randn(*s, dtype=torch.bfloat16) * 3.2)(D, Hq * Dh) * (D ** -0.5)),
            "wo": _w((lambda *s: torch.randn(*s, dtype=torch.bfloat16) * 0.8)(Hq * Dh, D) * ((Hq * Dh) ** -0.5)),
        }
    _A_in = _A_setup()

    def _B_setup():
        B, S, D, F = 4, 1, 512, 1792
        return {
            "x": _w((lambda *s: torch.randn(*s, dtype=torch.bfloat16) * 0.05)(B, S, D)),
            "w1": _w((lambda *s: torch.randn(*s, dtype=torch.bfloat16) * 3.2)(D, F) * (D ** -0.5)),
            "w2": _w((lambda *s: torch.randn(*s, dtype=torch.bfloat16) * 0.8)(F, D) * (F ** -0.5)),
        }
    _B_in = _B_setup()

    return {
        "A_x": _A_in["x"],
        "A_k_cache": _A_in["k_cache"],
        "A_v_cache": _A_in["v_cache"],
        "A_wq": _A_in["wq"],
        "A_wo": _A_in["wo"],
        "B_x": _B_in["x"],
        "B_w1": _B_in["w1"],
        "B_w2": _B_in["w2"],
    }

def reference(A_x, A_k_cache, A_v_cache, A_wq, A_wo, B_x, B_w1, B_w2):
    def _A_ref(x, k_cache, v_cache, wq, wo):
        B, Sq, D = x.shape
        Hq, Dh = 32, 128
        Hkv, Sk = k_cache.shape[1], k_cache.shape[2]
        W = min(4096, max(256, Sk // 4))
        rep = Hq // Hkv
        q = (lambda x, *d: x.permute(*d))((x @ wq).reshape(B, Sq, Hq, Dh), 0, 2, 1, 3)
        k = (lambda x, n, axis: torch.repeat_interleave(x, n, dim=axis))(k_cache[:, :, Sk - W:, :], rep, axis=1)
        v = (lambda x, n, axis: torch.repeat_interleave(x, n, dim=axis))(v_cache[:, :, Sk - W:, :], rep, axis=1)
        scores = torch.einsum("bhqd,bhkd->bhqk", q, k) * (Dh ** -0.5)
        p = _softmax(scores, axis=-1)
        o = torch.einsum("bhqk,bhkd->bhqd", p, v)
        return (lambda x, *d: x.permute(*d))(o, 0, 2, 1, 3).reshape(B, Sq, Hq * Dh) @ wo

    def _B_ref(x, w1, w2):
        h = torch.einsum("bsd,df->bsf", x, w1)
        h = _silu(h)
        out = torch.einsum("bsf,fd->bsd", h, w2)
        return out

    x = A_x
    h = x + _A_ref(_layernorm(x, axis=-1), A_k_cache, A_v_cache, A_wq, A_wo)
    h2 = h + _B_ref(_layernorm(h, axis=-1), B_w1, B_w2)
    return h2
```

```cpp
#define DLY3 20
#define W2_AT 2
#define STAMPS
#define SIDE_WG 8
#define SIDE_WAVE 0
#define SIDE_IDX 1
#include <hip/hip_runtime.h>
#include <hip/hip_bf16.h>
#include <stdint.h>

typedef unsigned u32;
typedef unsigned long long u64;
typedef u32 v4u __attribute__((ext_vector_type(4)));
typedef u32 v2u __attribute__((ext_vector_type(2)));
typedef float v4f __attribute__((ext_vector_type(4)));
typedef float v2f __attribute__((ext_vector_type(2)));
typedef __bf16 bf16x8 __attribute__((ext_vector_type(8)));
typedef __bf16 bf16x2 __attribute__((ext_vector_type(2)));
typedef short s16x4 __attribute__((ext_vector_type(4)));
typedef __amdgpu_buffer_rsrc_t rsrc_t;

constexpr int D = 512, HKV = 8, SK = 16384, DH = 128, WIN = 4096, FF = 1792, QD = 4096;
constexpr int NWG = 256, NFF = 56;
constexpr u32 SENT = 0xFFFFFFFFu;
constexpr int AUX_NT = 2, AUX_SC1 = 16;
#define NUMA 0
#define HINT1_AT 14
#define HINT2_EARLY 1
#define DLY1 0
#define DLY2 0
#define PRE_K 4
#define WTHR 6
#define QDLY 20
#define H1_SPLIT 8
constexpr int NST = 6;
constexpr int OOB = 0x7FFFFF00;
constexpr u32 SPIN_CAP = 1u << 20;
constexpr float LOG2E = 1.4426950408889634f;

constexpr int A_H1 = 0;
constexpr int A_H2 = A_H1 + NWG * 2048;
constexpr int A_ST = A_H2 + 32 * 2048;
constexpr int A_HN = A_ST + NWG * 128;
constexpr int A_H3 = A_HN + 32 * 256;
constexpr int A_END = A_H3 + 4 * NFF * 2048;
constexpr int WS_E = 2 * ((A_END + 16383) / 16384) * 16384;
constexpr int WS_FLAG = WS_E + 4096;
constexpr int WS_QP = WS_FLAG + 4096;
constexpr int WS_END = WS_QP + 8 * 4 * 4 * 2048;
constexpr int WS_DBG = 8 << 20;
__device__ __forceinline__ int ph(int o, int side) { const int p = ((o >> 13) << 14) | (o & 8191); return p | (((side ^ (p >> 21)) & 1) << 13); }

constexpr int L_Q = 0;
constexpr int L_FLAG = 1024;
constexpr int L_ST = 1088;
constexpr int L_ACC = 2048;
constexpr int L_RB = L_ACC + 16384;
constexpr int L_RC = L_RB + 2048;
constexpr int L_CS = L_RC + 2048 + 64;
constexpr int L_LN = L_CS + 2048 + 64;
constexpr int L_H = L_LN + 64;
constexpr int L_END = L_H + 1024;

#define STAMP(i) do { FENCE(); asm volatile("s_memrealtime %0\n\ts_waitcnt lgkmcnt(0)" : "=s"(st_[i]) :: "memory"); FENCE(); } while (0)

__device__ __forceinline__ rsrc_t mk_rsrc(const void* p, u32 bytes) { return __builtin_amdgcn_make_buffer_rsrc((void*)p, 0, bytes, 0x00020000); }
__device__ __forceinline__ float f_of(u32 x) { return __builtin_bit_cast(float, x); }
__device__ __forceinline__ u32 u_of(float x) { return __builtin_bit_cast(u32, x); }
__device__ __forceinline__ u32 pk_bf16(float lo, float hi) { v2f t = {lo, hi}; return __builtin_bit_cast(u32, __builtin_convertvector(t, bf16x2)); }
__device__ __forceinline__ float bf_lo(u32 x) { return f_of(x << 16); }
__device__ __forceinline__ float bf_hi(u32 x) { return f_of(x & 0xFFFF0000u); }
__device__ __forceinline__ float rbf(float x) { return bf_lo(pk_bf16(x, x)); }
__device__ __forceinline__ u32 san(float x) { u32 u = u_of(x); return u < SENT ? u : SENT - 1u; }
__device__ __forceinline__ u32 umax(u32 a, u32 b) { return a > b ? a : b; }
__device__ __forceinline__ u32 vmax(v4u v) { return umax(umax(v.x, v.y), umax(v.z, v.w)); }
__device__ __forceinline__ bool unset(v4u v) { return umax(umax(v.x, v.y), umax(v.z, v.w)) == SENT; }
template <int CTRL> __device__ __forceinline__ u32 dppu(u32 x) { return (u32)__builtin_amdgcn_mov_dpp((int)x, CTRL, 0xf, 0xf, true); }
template <int CTRL> __device__ __forceinline__ float dppf(float x) { return f_of(dppu<CTRL>(u_of(x))); }
constexpr int QP_X1 = 0xB1, QP_X2 = 0x4E, ROW_ROR4 = 0x124, ROW_ROR8 = 0x128, ROW_HMIRROR = 0x141;
constexpr int ROW_SHL(int n) { return 0x100 + n; }
__device__ __forceinline__ float x16_add(float x) { auto s = __builtin_amdgcn_permlane16_swap(u_of(x), u_of(x), false, false); return f_of(s[0]) + f_of(s[1]); }
__device__ __forceinline__ float x32_add(float x) { auto s = __builtin_amdgcn_permlane32_swap(u_of(x), u_of(x), false, false); return f_of(s[0]) + f_of(s[1]); }
__device__ __forceinline__ float x16_max(float x) { auto s = __builtin_amdgcn_permlane16_swap(u_of(x), u_of(x), false, false); return fmaxf(f_of(s[0]), f_of(s[1])); }
__device__ __forceinline__ float x32_max(float x) { auto s = __builtin_amdgcn_permlane32_swap(u_of(x), u_of(x), false, false); return fmaxf(f_of(s[0]), f_of(s[1])); }
__device__ __forceinline__ float t32_add(float a, float b) { auto s = __builtin_amdgcn_permlane32_swap(u_of(a), u_of(b), false, false); return f_of(s[0]) + f_of(s[1]); }
__device__ __forceinline__ float t16_add(float a, float b) { auto s = __builtin_amdgcn_permlane16_swap(u_of(a), u_of(b), false, false); return f_of(s[0]) + f_of(s[1]); }
__device__ __forceinline__ float row16_sum(float x) { x += dppf<QP_X1>(x); x += dppf<QP_X2>(x); x += dppf<ROW_ROR4>(x); x += dppf<ROW_ROR8>(x); return x; }
__device__ __forceinline__ v4f mfma32(v4u a, v4u b, v4f c) { return __builtin_amdgcn_mfma_f32_16x16x32_bf16(__builtin_bit_cast(bf16x8, a), __builtin_bit_cast(bf16x8, b), c, 0, 0, 0); }
__device__ __forceinline__ v4f mfma16(v2u a, v2u b, v4f c) { return __builtin_amdgcn_mfma_f32_16x16x16bf16_1k(__builtin_bit_cast(s16x4, a), __builtin_bit_cast(s16x4, b), c, 0, 0, 0); }
#define FENCE() __builtin_amdgcn_sched_barrier(0)
#define CLOB() asm volatile("" ::: "memory")
__device__ __forceinline__ void bar() { FENCE(); asm volatile("s_waitcnt lgkmcnt(0)\n\ts_barrier" ::: "memory"); FENCE(); }
typedef volatile __attribute__((address_space(3))) u32 lds_vu32;
__device__ __forceinline__ int vzero() { int z; asm volatile("v_mov_b32 %0, 0" : "=v"(z)); return z; }
template <int N> __device__ __forceinline__ void pause() { if (N > 0) { FENCE(); __builtin_amdgcn_s_sleep(N); FENCE(); } }
#define LD4(r, v, s, aux) __builtin_amdgcn_raw_buffer_load_b128(r, v, s, aux)
#define ST4(d, r, v, s) __builtin_amdgcn_raw_buffer_store_b128(d, r, v, s, AUX_SC1)

__device__ __forceinline__ void tr8(const v4u (&w)[8], v4u (&f)[8]) {
#pragma unroll
  for (int t = 0; t < 8; ++t)
#pragma unroll
    for (int i = 0; i < 4; ++i) f[t][i] = __builtin_amdgcn_perm(w[2 * i + 1][t >> 1], w[2 * i][t >> 1], (t & 1) ? 0x07060302u : 0x05040100u);
}

__device__ __forceinline__ u64 next_epoch(u64 e) {
  u64 z = e * 0x9E3779B97F4A7C15ull + 0xD1B54A32D192ED03ull;
  z ^= z >> 29; z *= 0xBF58476D1CE4E5B9ull; z ^= z >> 32;
  return (z & ~1ull) | (~e & 1ull);
}

__device__ __forceinline__ void lds_wait(const char* p) {
#pragma unroll 1
  for (u32 spins = 0; spins < SPIN_CAP * 8u; ++spins) {
    if (__builtin_amdgcn_readfirstlane((int)*(lds_vu32*)p) != 0) break;
    __builtin_amdgcn_s_sleep(1);
  }
  CLOB();
}

__device__ __forceinline__ void gather4(rsrc_t rs, const int (&a)[4], v4u (&v)[4]) {
#pragma unroll
  for (int i = 0; i < 4; ++i) v[i] = LD4(rs, a[i], 0, AUX_SC1);
#pragma unroll 1
  for (u32 spins = 0; spins < SPIN_CAP; ++spins) {
    const u32 m0 = umax(umax(v[0].x, v[0].y), umax(v[0].z, v[0].w)), m1 = umax(umax(v[1].x, v[1].y), umax(v[1].z, v[1].w));
    const u32 m2 = umax(umax(v[2].x, v[2].y), umax(v[2].z, v[2].w)), m3 = umax(umax(v[3].x, v[3].y), umax(v[3].z, v[3].w));
    if (__builtin_expect(__builtin_amdgcn_ballot_w64(umax(umax(m0, m1), umax(m2, m3)) == SENT) == 0ull, 1)) break;
    CLOB();
    if (m0 == SENT) v[0] = LD4(rs, a[0], 0, AUX_SC1);
    if (m1 == SENT) v[1] = LD4(rs, a[1], 0, AUX_SC1);
    if (m2 == SENT) v[2] = LD4(rs, a[2], 0, AUX_SC1);
    if (m3 == SENT) v[3] = LD4(rs, a[3], 0, AUX_SC1);
  }
}
__device__ __forceinline__ v2u poll2(rsrc_t rs, int a) {
  v2u v;
#pragma unroll 1
  for (u32 spins = 0; spins < SPIN_CAP; ++spins) {
    CLOB();
    v = __builtin_amdgcn_raw_buffer_load_b64(rs, a, 0, AUX_SC1);
    if (__builtin_amdgcn_ballot_w64(umax(v.x, v.y) == SENT) == 0ull) break;
  }
  CLOB();
  return v;
}

__global__ void __launch_bounds__(512, 2)
fused_decode_kernel(const u32* __restrict__ x, const char* __restrict__ kc, const char* __restrict__ vc, const char* __restrict__ wq,
                    const char* __restrict__ wo, const char* __restrict__ w1, const char* __restrict__ w2, u32* __restrict__ out, char* ws, u64 pol) {
  __shared__ __attribute__((aligned(16))) char lds[L_END];
  u64 st_[16];
#pragma unroll
  for (int i = 0; i < 16; ++i) st_[i] = 0;
  STAMP(0);
  if (threadIdx.x < 8) *(lds_vu32*)(lds + L_FLAG + threadIdx.x * 4) = 0u;
  bar();
  asm volatile("" :: "s"(x), "s"(kc), "s"(vc), "s"(wq), "s"(wo), "s"(w1), "s"(w2), "s"(out), "s"(ws), "s"(pol));
  const int wg = blockIdx.x, tid = threadIdx.x, lane = tid & 63;
  const int wave = __builtin_amdgcn_readfirstlane(tid >> 6);
  const int kvh = wg & 7, s = (wg >> 3) & 7, b = wg >> 6;
  const int g = lane >> 4, n = lane & 15, half = lane >> 5;
  const rsrc_t rws = mk_rsrc(ws, WS_END);
  const size_t kvbase = ((size_t)(b * HKV + kvh) * SK + (SK - WIN) + s * 512) * (DH * 2);
  const rsrc_t rk = mk_rsrc(kc + kvbase, 512 * 256), rv = mk_rsrc(vc + kvbase, 512 * 256);
  const int c0 = wave * 16 + (wg >> 3) * 4;
  const int koff = ((lane >> 2) & 3) * 256 + g * 64 + (lane & 3) * 16, voff = lane * 16;
  const int ug = kvh * 7 + s;
  const int grp = kvh * 4 + b;
  auto side = [&](int xcd, int o) { return NUMA ? ((xcd >> 2) ^ (int)pol) & 1 : (o >> 11) & 1; };

  v4u K[16], V[16];

  if (wave == 7) {
    __builtin_amdgcn_s_setprio(3);
    const int j = wg >> 3, kq = j & 3, cg = j >> 2;
    const v2u e2 = __builtin_amdgcn_raw_buffer_load_b64(rws, vzero(), WS_E, AUX_SC1);
    const rsrc_t rx = mk_rsrc(x, 4 * D * 2);
    v4u xs[4], xa[2];
#pragma unroll
    for (int i = 0; i < 4; ++i) xs[i] = LD4(rx, (n >> 2) * 1024 + ((n & 3) + g * 4) * 16, i * 256, 0);
#pragma unroll
    for (int u = 0; u < 2; ++u) xa[u] = LD4(rx, (n >> 2) * 1024 + ((n & 1) * 64 + g * 8) * 2, (kq * 128 + u * 32) * 2, 0);
    const rsrc_t rq = mk_rsrc(wq + ((size_t)kq * 128 * QD + kvh * 512 + cg * 64) * 2, 128 * QD * 2);
    v4u wr[2][8];
#pragma unroll
    for (int u = 0; u < 2; ++u)
#pragma unroll
      for (int r = 0; r < 8; ++r) wr[u][r] = LD4(rq, ((n >> 3) * 64 + g * 8) * (QD * 2) + (n & 7) * 16, (u * 32 + r) * (QD * 2), AUX_NT);
    const v4u sv = {SENT, SENT, SENT, SENT};
    const int qoff = WS_QP + ((kvh * 4 + g) * 4 + kq) * 2048 + cg * 256 + (n & 7) * 32 + (n >> 3) * 16;
    ST4(sv, rws, qoff, 0);
    { const int o = A_H1 + (grp * 8 + s) * 2048 + lane * 16, p = ph(o, side(kvh, o)); ST4(sv, rws, p, 0); ST4(sv, rws, p, 1024); }
    {
      const int o2 = A_H2 + (b * 8 + kvh) * 2048 + s * 256 + n * 16, o3 = A_ST + (grp * 8 + s) * 128 + (n & 1) * 16, o4 = A_HN + (b * 8 + kvh) * 256;
      const int p = g < 2 ? ph(o2, NUMA ? g : side(0, o2)) : g == 2 ? ph(o3, side(kvh, o3)) : ph(o4, NUMA ? n : side(0, o4));
      ST4(sv, rws, (g < 2 || (n < 2 && (g == 2 || s == 0))) ? p : OOB, 0);
    }
    {
#pragma unroll
      for (int h = 0; h < 2; ++h) { const int o = A_H3 + (b * NFF + ug) * 2048 + h * 1024 + lane * 16; ST4(sv, rws, s < 7 ? ph(o, side(h * 4, o)) : OOB, 0); }
    }
    FENCE();
    STAMP(1);
    const u64 epoch = next_epoch(((u64)e2.y << 32) | e2.x);
    const v2u ep = {(u32)epoch, (u32)(epoch >> 32)};
    float sum = 0.f;
#pragma unroll
    for (int i = 0; i < 4; ++i)
#pragma unroll
      for (int r = 0; r < 4; ++r) sum += bf_lo(xs[i][r]) + bf_hi(xs[i][r]);
    sum += dppf<QP_X1>(sum); sum += dppf<QP_X2>(sum); sum = x32_add(x16_add(sum));
    const float mean = sum * (1.f / D);
    float sq = 0.f;
#pragma unroll
    for (int i = 0; i < 4; ++i)
#pragma unroll
      for (int r = 0; r < 4; ++r) { const float a = bf_lo(xs[i][r]) - mean, c = bf_hi(xs[i][r]) - mean; sq += a * a + c * c; }
    sq += dppf<QP_X1>(sq); sq += dppf<QP_X2>(sq); sq = x32_add(x16_add(sq));
    const float mb = rbf(mean), rs = __builtin_amdgcn_rcpf(rbf(__builtin_amdgcn_sqrtf(rbf(rbf(sq * (1.f / D)) + 1e-5f))));
    v4u an[2];
#pragma unroll
    for (int u = 0; u < 2; ++u)
#pragma unroll
      for (int r = 0; r < 4; ++r) an[u][r] = pk_bf16(rbf(bf_lo(xa[u][r]) - mb) * rs, rbf(bf_hi(xa[u][r]) - mb) * rs);
    FENCE();
    asm volatile("s_waitcnt vmcnt(0)" ::: "memory");
    if (lane == 0) __builtin_amdgcn_raw_buffer_store_b64(ep, rws, 0, WS_FLAG + wg * 8, AUX_SC1);
    STAMP(2);
    v4f qa[8];
#pragma unroll
    for (int t = 0; t < 8; ++t) qa[t] = v4f{0.f, 0.f, 0.f, 0.f};
#pragma unroll
    for (int u = 0; u < 2; ++u) {
      v4u f[8]; tr8(wr[u], f);
#pragma unroll
      for (int t = 0; t < 8; ++t) qa[t] = mfma32(an[u], f[t], qa[t]);
    }
    v4u qo;
#pragma unroll
    for (int i = 0; i < 4; ++i) { const float lo = qa[i][0] + dppf<ROW_ROR8>(qa[i][1]), hi = qa[4 + i][1] + dppf<ROW_ROR8>(qa[4 + i][0]); qo[i] = san(n < 8 ? lo : hi); }
    ST4(qo, rws, qoff, 0);
    STAMP(3);
    pause<QDLY>();
#pragma unroll 1
    for (u32 spins = 0; spins < SPIN_CAP; ++spins) {
      CLOB();
      const v4u f0 = LD4(rws, lane * 16, WS_FLAG, AUX_SC1), f1 = LD4(rws, lane * 16, WS_FLAG + 1024, AUX_SC1);
      const bool bad = ((f0.x ^ ep.x) | (f0.y ^ ep.y) | (f0.z ^ ep.x) | (f0.w ^ ep.y) | (f1.x ^ ep.x) | (f1.y ^ ep.y) | (f1.z ^ ep.x) | (f1.w ^ ep.y)) != 0u;
      if (__builtin_amdgcn_ballot_w64(bad) == 0ull) break;
    }
    CLOB();
    STAMP(14);
    if (wg == 0 && lane == 0) __builtin_amdgcn_raw_buffer_store_b64(ep, rws, 0, WS_E, AUX_SC1);
    v4u qp[2][4];
#pragma unroll 1
    for (u32 spins = 0; spins < SPIN_CAP; ++spins) {
      CLOB();
      bool bad = false;
#pragma unroll
      for (int i = 0; i < 2; ++i)
#pragma unroll
        for (int k = 0; k < 4; ++k) qp[i][k] = LD4(rws, lane * 16, WS_QP + (grp * 4 + k) * 2048 + i * 1024, AUX_SC1);
#pragma unroll
      for (int i = 0; i < 2; ++i)
#pragma unroll
        for (int k = 0; k < 4; ++k) bad |= unset(qp[i][k]);
      if (__builtin_amdgcn_ballot_w64(bad) == 0ull) break;
    }
#pragma unroll
    for (int i = 0; i < 2; ++i) {
      float q4[4];
#pragma unroll
      for (int r = 0; r < 4; ++r) q4[r] = (f_of(qp[i][0][r]) + f_of(qp[i][1][r])) + (f_of(qp[i][2][r]) + f_of(qp[i][3][r]));
      *(v2u*)(lds + L_Q + i * 512 + lane * 8) = v2u{pk_bf16(q4[0], q4[1]), pk_bf16(q4[2], q4[3])};
    }
    asm volatile("s_waitcnt lgkmcnt(0)" ::: "memory");
    if (lane == 0) *(lds_vu32*)(lds + L_FLAG + 16) = 1u;
    __builtin_amdgcn_s_setprio(0);
    STAMP(11);
  }
  if (wave < 4) {
#pragma unroll
    for (int i = 0; i < 32; ++i) {
      if (i == PRE_K) { STAMP(1); asm volatile("s_waitcnt vmcnt(%0)" :: "i"(PRE_K - 1) : "memory"); STAMP(2); }
      if (i == 16) STAMP(3);
      if (i > WTHR) asm volatile("s_waitcnt vmcnt(%0)" :: "i"(WTHR - 1) : "memory");
      if (i < 16) K[i] = LD4(rk, koff, ((c0 + i) & 127) * 1024, AUX_NT);
      else V[i - 16] = LD4(rv, voff, ((c0 + i - 16) & 127) * 1024, AUX_NT);
      FENCE();
    }
    *(lds_vu32*)(lds + L_FLAG + wave * 4) = 1u;
  } else {
    lds_wait(lds + L_FLAG + (wave - 4) * 4);
#pragma unroll
    for (int i = 0; i < 16; ++i) K[i] = LD4(rk, koff, ((c0 + i) & 127) * 1024, AUX_NT);
#pragma unroll
    for (int i = 0; i < 16; ++i) V[i] = LD4(rv, voff, ((c0 + i) & 127) * 1024, AUX_NT);
  }
  FENCE();
  STAMP(4);

  lds_wait(lds + L_FLAG + 16);
  STAMP(5);

  float E[4], mloc;
  {
    const v4u qb = *(const v4u*)(lds + L_Q + (n >> 2) * 256 + g * 64 + (n & 3) * 16);
    v4f d[16];
#pragma unroll
    for (int i = 0; i < 16; ++i) d[i] = mfma32(K[i], qb, v4f{0.f, 0.f, 0.f, 0.f});
    FENCE();
    float t[16];
#pragma unroll
    for (int i = 0; i < 16; ++i) t[i] = (i & 3) == 0 ? d[i][0] + dppf<0x55>(d[i][1]) : d[i][i & 3] + dppf<0x00>(d[i][0]);
    FENCE();
#pragma unroll
    for (int i = 0; i < 16; ++i) t[i] += (i & 3) < 2 ? dppf<0xAA>(d[i][2]) : dppf<0x55>(d[i][1]);
    FENCE();
#pragma unroll
    for (int i = 0; i < 16; ++i) t[i] += (i & 3) < 3 ? dppf<0xFF>(d[i][3]) : dppf<0xAA>(d[i][2]);
    FENCE();
    float S[4];
#pragma unroll
    for (int k = 0; k < 4; ++k) {
      const float t0 = t[4 * k], t1 = t[4 * k + 1], t2 = t[4 * k + 2], t3 = t[4 * k + 3];
      const float a = (lane & 1) ? t1 : t0, c = (lane & 1) ? t3 : t2;
      S[k] = rbf(rbf((lane & 2) ? c : a) * 0.08838834764831845f);
    }
    float m = fmaxf(fmaxf(S[0], S[1]), fmaxf(S[2], S[3]));
    m = fmaxf(m, dppf<QP_X1>(m)); m = fmaxf(m, dppf<QP_X2>(m)); m = x32_max(x16_max(m));
    float l = 0.f;
#pragma unroll
    for (int k = 0; k < 4; ++k) { E[k] = __builtin_amdgcn_exp2f((S[k] - m) * LOG2E); l += E[k]; }
    l += dppf<QP_X1>(l); l += dppf<QP_X2>(l); l = x32_add(x16_add(l));
    if (lane < 16 && (lane & 3) == 0) *(v2f*)(lds + L_ST + (lane >> 2) * 64 + wave * 8) = v2f{m, l};
    mloc = m;
  }
  auto pv = [&](float f, v4f& o0, v4f& o1, auto&& mid) {
    const u32 m0 = (lane & 3) == 0 ? 0x0000FFFFu : (lane & 3) == 1 ? 0xFFFF0000u : 0u;
    const u32 m1 = (lane & 3) == 2 ? 0x0000FFFFu : (lane & 3) == 3 ? 0xFFFF0000u : 0u;
    u32 P[4];
#pragma unroll
    for (int k = 0; k < 4; ++k) P[k] = pk_bf16(E[k] * f, E[k] * f);
    v2u A[16];
#pragma unroll
    for (int i = 0; i < 16; ++i) {
      const u32 pb = (i & 3) == 0 ? dppu<0x00>(P[i >> 2]) : (i & 3) == 1 ? dppu<0x55>(P[i >> 2]) : (i & 3) == 2 ? dppu<0xAA>(P[i >> 2]) : dppu<0xFF>(P[i >> 2]);
      A[i] = v2u{pb & m0, pb & m1};
    }
    FENCE();
    o0 = v4f{0.f, 0.f, 0.f, 0.f}; o1 = o0;
#pragma unroll
    for (int i = 0; i < 16; ++i) {
      if (i == HINT1_AT) { FENCE(); mid(); FENCE(); }
      o0 = mfma16(A[i], v2u{V[i].x, V[i].y}, o0);
      o1 = mfma16(A[i], v2u{V[i].z, V[i].w}, o1);
    }
    if (HINT1_AT >= 16) { FENCE(); mid(); FENCE(); }
  };
  auto wg_stats = [&](int h, float& M, float& L) {
    v4f q[4];
#pragma unroll
    for (int i = 0; i < 4; ++i) q[i] = *(const v4f*)(lds + L_ST + h * 64 + i * 16);
    M = fmaxf(fmaxf(fmaxf(q[0].x, q[0].z), fmaxf(q[1].x, q[1].z)), fmaxf(fmaxf(q[2].x, q[2].z), fmaxf(q[3].x, q[3].z)));
    L = 0.f;
#pragma unroll
    for (int i = 0; i < 4; ++i) { L = fmaf(__builtin_amdgcn_exp2f((q[i].x - M) * LOG2E), q[i].y, L); L = fmaf(__builtin_amdgcn_exp2f((q[i].z - M) * LOG2E), q[i].w, L); }
  };

  if (wave < 4) {
    v4f o0, o1;
    pv(1.f, o0, o1, [] {});
    STAMP(6);
    v4u fa[8];
    u32 xp = 0;
    const bool dv = (n >> 2) == g;
    const int rowb = (g & 1) * 64 + (g >> 1) * 2;
    if (s < 7) {
      const rsrc_t r1 = mk_rsrc(w1 + ((size_t)(wave * 128) * FF + ug * 32) * 2, 128 * FF * 2 - ug * 64);
      v4u wa[8];
#pragma unroll
      for (int r = 0; r < 8; ++r) wa[r] = LD4(r1, (rowb + (n >> 2) * 16) * (FF * 2) + (n & 3) * 16, ((r >> 1) * 4 + (r & 1)) * (FF * 2), AUX_NT);
      xp = x[(b * D + wave * 128 + (lane & 31) * 4 + half * 2) / 2];
      FENCE();
      tr8(wa, fa);
      const v4u ones = {0x3F803F80u, 0x3F803F80u, 0x3F803F80u, 0x3F803F80u};
      v4f cs[8];
#pragma unroll
      for (int t = 0; t < 8; ++t) cs[t] = mfma32(ones, fa[t], v4f{0.f, 0.f, 0.f, 0.f});
      if (dv) {
        float* p = (float*)(lds + L_CS + (wave * 4 + g) * 128 + (n & 3) * 32);
#pragma unroll
        for (int t = 0; t < 8; ++t) p[t] = cs[t][0];
      }
    } else {
#pragma unroll
      for (int t = 0; t < 8; ++t) fa[t] = v4u{0u, 0u, 0u, 0u};
    }
    int a1[4], a2[4];
    const int cpos = (lane & 24) | ((lane + 2 * b) & 7);
#pragma unroll
    for (int i = 0; i < 4; ++i) {
      const int o = A_H1 + (grp * 8 + 2 * i + half) * 2048 + wave * 512 + cpos * 16; a1[i] = ph(o, side(kvh, o));
      const int o2 = A_H2 + (b * 8 + 2 * i + half) * 2048 + wave * 512 + (lane & 31) * 16; a2[i] = ph(o2, side(kvh, o2));
    }
    const int ost = A_ST + (grp * 8 + (lane & 7)) * 128 + wave * 8; int ast = ph(ost, side(kvh, ost));
    const int ohn = A_HN + (b * 8 + (lane & 7)) * 256; int ahn = ph(ohn, side(kvh, ohn));
    const int ohw = A_HN + (b * 8 + kvh) * 256; int ahw = (wave == 0 && s == 0 && lane < (NUMA ? 2 : 1)) ? ph(ohw, NUMA ? lane : side(0, ohw)) : OOB;
    const int o2w = A_H2 + (b * 8 + kvh) * 2048 + s * 256 + n * 16; int a2w = lane < (NUMA ? 32 : 16) ? ph(o2w, NUMA ? g : side(0, o2w)) : OOB;
    const int col = (kvh * 4 + (s & 3)) * 16;
    int a3[4];
#pragma unroll
    for (int i = 0; i < 4; ++i) { const int o = A_H3 + (b * NFF + i * 14 + (lane >> 2)) * 2048 + col * 4 + (lane & 3) * 16; a3[i] = lane < 56 ? ph(o, side(kvh, o)) : OOB; }
    int aout = n < 4 ? (b * D + col + n * 4 + g) * 2 : OOB;
    const rsrc_t rout = mk_rsrc(out, 4 * D * 2);
#pragma unroll
    for (int i = 0; i < 4; ++i) asm volatile("" : "+v"(a1[i]), "+v"(a2[i]), "+v"(a3[i]));
    asm volatile("" : "+v"(ast), "+v"(ahn), "+v"(ahw), "+v"(a2w), "+v"(aout));
    STAMP(7);
    bar();
    {
      float M, L;
      wg_stats(g, M, L);
      const float f = __builtin_amdgcn_exp2f((*(const float*)(lds + L_ST + g * 64 + wave * 8) - M) * LOG2E);
      *(v4f*)(lds + L_ACC + wave * 2048 + lane * 32) = o0 * f;
      *(v4f*)(lds + L_ACC + wave * 2048 + lane * 32 + 16) = o1 * f;
    }
    FENCE();
    const rsrc_t ro = mk_rsrc(wo + ((size_t)(kvh * 512 + wave * 128) * D + s * 64) * 2, 128 * D * 2 - s * 128);
    const int wooff = (rowb + (n >> 3) * 32) * (D * 2) + (n & 7) * 16;
    v4u wr[2][8];
#pragma unroll
    for (int r = 0; r < 8; ++r) wr[0][r] = LD4(ro, wooff, ((((r >> 1) + 2 * b) & 7) * 4 + (r & 1)) * (D * 2), AUX_NT);
    bar();
#pragma unroll
    for (int r = 0; r < 8; ++r) wr[1][r] = LD4(ro, wooff, (((4 + (r >> 1) + 2 * b) & 7) * 4 + (r & 1)) * (D * 2), AUX_NT);
    FENCE();
    STAMP(8);
    v4u f0[8], f1[8];
    tr8(wr[0], f0);
#pragma unroll
    for (int t = 0; t < 8; ++t) asm volatile("" : "+v"(f0[t]));
    const v2u stt = poll2(rws, ast);
    if (HINT2_EARLY) __builtin_amdgcn_raw_buffer_store_b64(v2u{1u, 1u}, rws, ahw, 0, AUX_SC1);
    STAMP(9);
    pause<DLY1>();
    v4u part[4];
#pragma unroll
    for (int i = 0; i < 4; ++i) part[i] = LD4(rws, a1[i], 0, AUX_SC1);
    FENCE();
    tr8(wr[1], f1);
    const float ms = f_of(stt.x);
    float M = fmaxf(ms, dppf<QP_X1>(ms)); M = fmaxf(M, dppf<QP_X2>(M)); M = fmaxf(M, dppf<ROW_HMIRROR>(M));
    const float fs = __builtin_amdgcn_exp2f((ms - M) * LOG2E);
    float L = fs * f_of(stt.y); L += dppf<QP_X1>(L); L += dppf<QP_X2>(L); L += dppf<ROW_HMIRROR>(L);
    const float wsn = fs * __builtin_amdgcn_rcpf(L);
    const float wsx = dppf<QP_X1>(wsn), wsh = half ? wsx : wsn;
    float wt[4] = {dppf<0x150>(wsh), dppf<0x152>(wsh), dppf<0x154>(wsh), dppf<0x156>(wsh)};
#pragma unroll
    for (int t = 0; t < 8; ++t) asm volatile("" : "+v"(f1[t]));
    asm volatile("" : "+v"(wt[0]), "+v"(wt[1]), "+v"(wt[2]), "+v"(wt[3]));
    FENCE();
    if (__builtin_expect(__builtin_amdgcn_ballot_w64(umax(umax(vmax(part[0]), vmax(part[1])), umax(vmax(part[2]), vmax(part[3]))) == SENT) != 0ull, 0)) gather4(rws, a1, part);
    if (!HINT2_EARLY) __builtin_amdgcn_raw_buffer_store_b64(v2u{1u, 1u}, rws, ahw, 0, AUX_SC1);
    STAMP(10);
    {
      v4f o = __builtin_bit_cast(v4f, part[0]) * wt[0];
#pragma unroll
      for (int i = 1; i < 4; ++i) o += __builtin_bit_cast(v4f, part[i]) * wt[i];
      const u32 op = pk_bf16(t32_add(o.x, o.z), t32_add(o.y, o.w));
      const v4u a0 = {op, dppu<ROW_SHL(1)>(op), dppu<ROW_SHL(2)>(op), dppu<ROW_SHL(3)>(op)};
      const v4u a1v = {dppu<ROW_SHL(4)>(op), dppu<ROW_SHL(5)>(op), dppu<ROW_SHL(6)>(op), dppu<ROW_SHL(7)>(op)};
      v4f dd[8];
#pragma unroll
      for (int t = 0; t < 8; ++t) dd[t] = mfma32(a1v, f1[t], mfma32(a0, f0[t], v4f{0.f, 0.f, 0.f, 0.f}));
      if (lane < 8 || (lane >= 40 && lane < 48)) {
        float* p = (float*)(lds + L_RB + (wave * 2 + half) * 256 + (lane & 7) * 32);
#pragma unroll
        for (int t = 0; t < 8; ++t) p[t] = dd[t][0];
      }
    }
    bar();
    if (wave == 0) {
      const v4f r0 = *(const v4f*)(lds + L_RB + (g * 2) * 256 + n * 16), r1 = *(const v4f*)(lds + L_RB + (g * 2 + 1) * 256 + n * 16);
      v4u o;
#pragma unroll
      for (int r = 0; r < 4; ++r) o[r] = san(x32_add(x16_add(r0[r] + r1[r])));
      ST4(o, rws, a2w, 0);
    }
    STAMP(11);
    if (s < 7) {
      poll2(rws, ahn);
      pause<DLY2>();
      gather4(rws, a2, part);
      STAMP(12);
      const v4f sm = (__builtin_bit_cast(v4f, part[0]) + __builtin_bit_cast(v4f, part[1])) + (__builtin_bit_cast(v4f, part[2]) + __builtin_bit_cast(v4f, part[3]));
      const u32 at = pk_bf16(t32_add(sm.x, sm.z), t32_add(sm.y, sm.w));
      const u32 hp = pk_bf16(bf_lo(xp) + bf_lo(at), bf_hi(xp) + bf_hi(at));
      const v4u a = {hp, dppu<ROW_SHL(1)>(hp), dppu<ROW_SHL(2)>(hp), dppu<ROW_SHL(3)>(hp)};
      v4f dd[8];
#pragma unroll
      for (int t = 0; t < 8; ++t) dd[t] = mfma32(a, fa[t], v4f{0.f, 0.f, 0.f, 0.f});
      *(u32*)(lds + L_H + wave * 256 + (lane & 31) * 8 + half * 4) = hp;
      const float cs0 = bf_lo((u32)__builtin_amdgcn_readfirstlane((int)hp));
      const float d0 = bf_lo(hp) - cs0, d1 = bf_hi(hp) - cs0;
      const float s12 = x16_add(row16_sum(t32_add(d0 + d1, d0 * d0 + d1 * d1)));
      if ((lane & 31) == 0) *(v2f*)(lds + L_LN + wave * 16 + half * 8) = v2f{s12, cs0};
      if (dv) {
        float* p = (float*)(lds + L_RC + (wave * 4 + g) * 128 + (n & 3) * 32);
#pragma unroll
        for (int t = 0; t < 8; ++t) p[t] = dd[t][0];
      }
      bar();
      STAMP(13);
      if (s < 4 && wave == 0) {
        const v2u hq = *(const v2u*)(lds + L_H + (col + (lane & 3) * 4) * 2);
        pause<DLY3>();
        gather4(rws, a3, part);
        STAMP(14);
        v4f r = (__builtin_bit_cast(v4f, part[0]) + __builtin_bit_cast(v4f, part[1])) + (__builtin_bit_cast(v4f, part[2]) + __builtin_bit_cast(v4f, part[3]));
        float y = t16_add(t32_add(r.x, r.z), t32_add(r.y, r.w));
        y += dppf<ROW_ROR4>(y); y += dppf<ROW_ROR8>(y);
        const u32 hw = half ? hq.y : hq.x;
        const float hv = (g & 1) ? bf_hi(hw) : bf_lo(hw);
        const u32 ob = pk_bf16(hv + rbf(y), 0.f);
        __builtin_amdgcn_raw_buffer_store_b16((unsigned short)ob, rout, aout, 0, 0);
      }
    }
  } else {
    const int w4 = wave - 4;
    v4u wb[8], fb[8];
    const rsrc_t r2 = mk_rsrc(w2 + ((size_t)(ug * 32) * D + w4 * 128) * 2, s < 7 ? 32 * D * 2 - w4 * 256 : 0);
    STAMP(6);
    bar();
    STAMP(7);
    float M, L;
    wg_stats(n >> 2, M, L);
    const int ost = A_ST + (grp * 8 + s) * 128 + (n >> 2) * 8;
    const int ast = (wave == 7 && lane < 16 && (lane & 3) == 0) ? ph(ost, side(kvh, ost)) : OOB;
    const int o1w = A_H1 + (grp * 8 + s) * 2048 + w4 * 512 + (lane & 31) * 16, a1t = ph(o1w, side(kvh, o1w));
    int a1w = lane < 32 ? a1t : OOB;
    asm volatile("" : "+v"(a1w));
    const v2u stv = {san(M), san(L)};
    v4f o0, o1;
    pv(__builtin_amdgcn_exp2f((mloc - M) * LOG2E), o0, o1, [&] {
      __builtin_amdgcn_raw_buffer_store_b64(stv, rws, ast, 0, AUX_SC1);
    });
    *(v4f*)(lds + L_ACC + wave * 2048 + lane * 32) = o0;
    *(v4f*)(lds + L_ACC + wave * 2048 + lane * 32 + 16) = o1;
    bar();
    STAMP(8);
    {
      v4f a[4];
#pragma unroll
      for (int i = 0; i < 4; ++i) a[i] = *(const v4f*)(lds + L_ACC + (half * 4 + i) * 2048 + w4 * 512 + (lane & 31) * 16);
      const v4f o = (a[0] + a[1]) + (a[2] + a[3]);
      v4u ou;
#pragma unroll
      for (int r = 0; r < 4; ++r) ou[r] = san(x32_add(o[r]));
      ST4(ou, rws, a1w, 0);
    }
    STAMP(9);
    if (W2_AT == 1) {
#pragma unroll
      for (int r = 0; r < 8; ++r) wb[r] = LD4(r2, (g * 8) * (D * 2) + n * 16, r * (D * 2), AUX_NT);
    }
    bar();
    if (s < 7) {
      if (W2_AT == 2) {
#pragma unroll
        for (int r = 0; r < 8; ++r) wb[r] = LD4(r2, (g * 8) * (D * 2) + n * 16, r * (D * 2), AUX_NT);
      }
      tr8(wb, fb);
      float csum;
      {
        float pp[8];
#pragma unroll
        for (int i = 0; i < 8; ++i) pp[i] = *(const float*)(lds + L_CS + ((lane & 8) + i) * 128 + (g * 8 + (lane & 7)) * 4);
        csum = ((pp[0] + pp[1]) + (pp[2] + pp[3])) + ((pp[4] + pp[5]) + (pp[6] + pp[7]));
        csum += dppf<ROW_ROR8>(csum);
      }
      int a3w[2];
#pragma unroll
      for (int h = 0; h < 2; ++h) { const int o = A_H3 + (b * NFF + ug) * 2048 + w4 * 512 + n * 32 + h * 16; a3w[h] = lane < 16 ? ph(o, side(w4 * 2, o)) : OOB; }
#pragma unroll
      for (int t = 0; t < 8; ++t) asm volatile("" : "+v"(fb[t]));
      asm volatile("" : "+v"(csum), "+v"(a3w[0]), "+v"(a3w[1]));
      STAMP(10);
      bar();
      STAMP(12);
      float pp[8];
#pragma unroll
      for (int i = 0; i < 8; ++i) pp[i] = *(const float*)(lds + L_RC + ((lane & 8) + i) * 128 + (g * 8 + (lane & 7)) * 4);
      const v4f ln = *(const v4f*)(lds + L_LN + (lane & 3) * 16);
      float mean = fmaf(ln.x, 1.f / D, ln.y * 0.25f);
      mean += dppf<QP_X1>(mean); mean += dppf<QP_X2>(mean);
      const float dm = mean - ln.y;
      float var = fmaf(dm, fmaf(128.f, dm, -2.f * ln.x), ln.z);
      var += dppf<QP_X1>(var); var += dppf<QP_X2>(var);
      const float mb = rbf(mean), rs = __builtin_amdgcn_rcpf(rbf(__builtin_amdgcn_sqrtf(rbf(rbf(fmaxf(var, 0.f) * (1.f / D)) + 1e-5f))));
      float ps = ((pp[0] + pp[1]) + (pp[2] + pp[3])) + ((pp[4] + pp[5]) + (pp[6] + pp[7]));
      ps += dppf<ROW_ROR8>(ps);
      const float pre = rbf(fmaf(-mb, csum, ps) * rs);
      const float h1 = pre * __builtin_amdgcn_rcpf(1.f + __builtin_amdgcn_exp2f(-pre * LOG2E));
      const u32 hq = pk_bf16(h1, dppf<ROW_SHL(1)>(h1));
      const v4u a2 = {hq, dppu<ROW_SHL(2)>(hq), dppu<ROW_SHL(4)>(hq), dppu<ROW_SHL(6)>(hq)};
      v4f dd[8];
#pragma unroll
      for (int t = 0; t < 8; ++t) dd[t] = mfma32(a2, fb[t], v4f{0.f, 0.f, 0.f, 0.f});
      ST4((v4u{san(dd[0][0]), san(dd[1][0]), san(dd[2][0]), san(dd[3][0])}), rws, a3w[0], 0);
      ST4((v4u{san(dd[4][0]), san(dd[5][0]), san(dd[6][0]), san(dd[7][0])}), rws, a3w[1], 0);
      STAMP(13);
    }
  }
  STAMP(15);
  if (wg == SIDE_WG && wave == SIDE_WAVE) {
    const u64 until = st_[15] + (st_[SIDE_IDX] - st_[0]) * 50ull;
    for (int w_ = 0; w_ < 400000; ++w_) { u64 t; asm volatile("s_memrealtime %0\n\ts_waitcnt lgkmcnt(0)" : "=s"(t) :: "memory"); if (t >= until) break; }
  }
}

constexpr int g_pol = 0;
extern "C" void kernel_launch(void* const* d_in, const int* in_sizes, int n_in, void* d_out, int out_size, void* d_ws, size_t ws_size,
                              hipStream_t stream) {
  fused_decode_kernel<<<NWG, 512, 0, stream>>>((const u32*)d_in[0], (const char*)d_in[1], (const char*)d_in[2], (const char*)d_in[3],
                                               (const char*)d_in[4], (const char*)d_in[6], (const char*)d_in[7], (u32*)d_out, (char*)d_ws, (u64)g_pol);
}
```

```cpp
#define DLY3 20
#define W2_AT 2
#include <hip/hip_runtime.h>
#include <hip/hip_bf16.h>
#include <stdint.h>

typedef unsigned u32;
typedef unsigned long long u64;
typedef u32 v4u __attribute__((ext_vector_type(4)));
typedef u32 v2u __attribute__((ext_vector_type(2)));
typedef float v4f __attribute__((ext_vector_type(4)));
typedef float v2f __attribute__((ext_vector_type(2)));
typedef __bf16 bf16x8 __attribute__((ext_vector_type(8)));
typedef __bf16 bf16x2 __attribute__((ext_vector_type(2)));
typedef short s16x4 __attribute__((ext_vector_type(4)));
typedef __amdgpu_buffer_rsrc_t rsrc_t;

constexpr int D = 512, HKV = 8, SK = 16384, DH = 128, WIN = 4096, FF = 1792, QD = 4096;
constexpr int NWG = 256, NFF = 56;
constexpr u32 SENT = 0xFFFFFFFFu;
constexpr int AUX_NT = 2, AUX_SC1 = 16;
#define NUMA 0
#define HINT1_AT 14
#define HINT2_EARLY 1
#define DLY1 0
#define DLY2 0
#define PRE_K 4
#define WTHR 6
#define QDLY 20
#define H1_SPLIT 8
constexpr int NST = 6;
constexpr int OOB = 0x7FFFFF00;
constexpr u32 SPIN_CAP = 1u << 20;
constexpr float LOG2E = 1.4426950408889634f;

constexpr int A_H1 = 0;
constexpr int A_H2 = A_H1 + NWG * 2048;
constexpr int A_ST = A_H2 + 32 * 2048;
constexpr int A_HN = A_ST + NWG * 128;
constexpr int A_H3 = A_HN + 32 * 256;
constexpr int A_END = A_H3 + 4 * NFF * 2048;
constexpr int WS_E = 2 * ((A_END + 16383) / 16384) * 16384;
constexpr int WS_FLAG = WS_E + 4096;
constexpr int WS_QP = WS_FLAG + 4096;
constexpr int WS_END = WS_QP + 8 * 4 * 4 * 2048;
constexpr int WS_DBG = 8 << 20;
__device__ __forceinline__ int ph(int o, int side) { const int p = ((o >> 13) << 14) | (o & 8191); return p | (((side ^ (p >> 21)) & 1) << 13); }

constexpr int L_Q = 0;
constexpr int L_FLAG = 1024;
constexpr int L_ST = 1088;
constexpr int L_ACC = 2048;
constexpr int L_RB = L_ACC + 16384;
constexpr int L_RC = L_RB + 2048;
constexpr int L_CS = L_RC + 2048 + 64;
constexpr int L_LN = L_CS + 2048 + 64;
constexpr int L_H = L_LN + 64;
constexpr int L_END = L_H + 1024;

#define STAMP(i) do {} while (0)

__device__ __forceinline__ rsrc_t mk_rsrc(const void* p, u32 bytes) { return __builtin_amdgcn_make_buffer_rsrc((void*)p, 0, bytes, 0x00020000); }
__device__ __forceinline__ float f_of(u32 x) { return __builtin_bit_cast(float, x); }
__device__ __forceinline__ u32 u_of(float x) { return __builtin_bit_cast(u32, x); }
__device__ __forceinline__ u32 pk_bf16(float lo, float hi) { v2f t = {lo, hi}; return __builtin_bit_cast(u32, __builtin_convertvector(t, bf16x2)); }
__device__ __forceinline__ float bf_lo(u32 x) { return f_of(x << 16); }
__device__ __forceinline__ float bf_hi(u32 x) { return f_of(x & 0xFFFF0000u); }
__device__ __forceinline__ float rbf(float x) { return bf_lo(pk_bf16(x, x)); }
__device__ __forceinline__ u32 san(float x) { u32 u = u_of(x); return u < SENT ? u : SENT - 1u; }
__device__ __forceinline__ u32 umax(u32 a, u32 b) { return a > b ? a : b; }
__device__ __forceinline__ u32 vmax(v4u v) { return umax(umax(v.x, v.y), umax(v.z, v.w)); }
__device__ __forceinline__ bool unset(v4u v) { return umax(umax(v.x, v.y), umax(v.z, v.w)) == SENT; }
template <int CTRL> __device__ __forceinline__ u32 dppu(u32 x) { return (u32)__builtin_amdgcn_mov_dpp((int)x, CTRL, 0xf, 0xf, true); }
template <int CTRL> __device__ __forceinline__ float dppf(float x) { return f_of(dppu<CTRL>(u_of(x))); }
constexpr int QP_X1 = 0xB1, QP_X2 = 0x4E, ROW_ROR4 = 0x124, ROW_ROR8 = 0x128, ROW_HMIRROR = 0x141;
constexpr int ROW_SHL(int n) { return 0x100 + n; }
__device__ __forceinline__ float x16_add(float x) { auto s = __builtin_amdgcn_permlane16_swap(u_of(x), u_of(x), false, false); return f_of(s[0]) + f_of(s[1]); }
__device__ __forceinline__ float x32_add(float x) { auto s = __builtin_amdgcn_permlane32_swap(u_of(x), u_of(x), false, false); return f_of(s[0]) + f_of(s[1]); }
__device__ __forceinline__ float x16_max(float x) { auto s = __builtin_amdgcn_permlane16_swap(u_of(x), u_of(x), false, false); return fmaxf(f_of(s[0]), f_of(s[1])); }
__device__ __forceinline__ float x32_max(float x) { auto s = __builtin_amdgcn_permlane32_swap(u_of(x), u_of(x), false, false); return fmaxf(f_of(s[0]), f_of(s[1])); }
__device__ __forceinline__ float t32_add(float a, float b) { auto s = __builtin_amdgcn_permlane32_swap(u_of(a), u_of(b), false, false); return f_of(s[0]) + f_of(s[1]); }
__device__ __forceinline__ float t16_add(float a, float b) { auto s = __builtin_amdgcn_permlane16_swap(u_of(a), u_of(b), false, false); return f_of(s[0]) + f_of(s[1]); }
__device__ __forceinline__ float row16_sum(float x) { x += dppf<QP_X1>(x); x += dppf<QP_X2>(x); x += dppf<ROW_ROR4>(x); x += dppf<ROW_ROR8>(x); return x; }
__device__ __forceinline__ v4f mfma32(v4u a, v4u b, v4f c) { return __builtin_amdgcn_mfma_f32_16x16x32_bf16(__builtin_bit_cast(bf16x8, a), __builtin_bit_cast(bf16x8, b), c, 0, 0, 0); }
__device__ __forceinline__ v4f mfma16(v2u a, v2u b, v4f c) { return __builtin_amdgcn_mfma_f32_16x16x16bf16_1k(__builtin_bit_cast(s16x4, a), __builtin_bit_cast(s16x4, b), c, 0, 0, 0); }
#define FENCE() __builtin_amdgcn_sched_barrier(0)
#define CLOB() asm volatile("" ::: "memory")
__device__ __forceinline__ void bar() { FENCE(); asm volatile("s_waitcnt lgkmcnt(0)\n\ts_barrier" ::: "memory"); FENCE(); }
typedef volatile __attribute__((address_space(3))) u32 lds_vu32;
__device__ __forceinline__ int vzero() { int z; asm volatile("v_mov_b32 %0, 0" : "=v"(z)); return z; }
template <int N> __device__ __forceinline__ void pause() { if (N > 0) { FENCE(); __builtin_amdgcn_s_sleep(N); FENCE(); } }
#define LD4(r, v, s, aux) __builtin_amdgcn_raw_buffer_load_b128(r, v, s, aux)
#define ST4(d, r, v, s) __builtin_amdgcn_raw_buffer_store_b128(d, r, v, s, AUX_SC1)

__device__ __forceinline__ void tr8(const v4u (&w)[8], v4u (&f)[8]) {
#pragma unroll
  for (int t = 0; t < 8; ++t)
#pragma unroll
    for (int i = 0; i < 4; ++i) f[t][i] = __builtin_amdgcn_perm(w[2 * i + 1][t >> 1], w[2 * i][t >> 1], (t & 1) ? 0x07060302u : 0x05040100u);
}

__device__ __forceinline__ u64 next_epoch(u64 e) {
  u64 z = e * 0x9E3779B97F4A7C15ull + 0xD1B54A32D192ED03ull;
  z ^= z >> 29; z *= 0xBF58476D1CE4E5B9ull; z ^= z >> 32;
  return (z & ~1ull) | (~e & 1ull);
}

__device__ __forceinline__ void lds_wait(const char* p) {
#pragma unroll 1
  for (u32 spins = 0; spins < SPIN_CAP * 8u; ++spins) {
    if (__builtin_amdgcn_readfirstlane((int)*(lds_vu32*)p) != 0) break;
    __builtin_amdgcn_s_sleep(1);
  }
  CLOB();
}

__device__ __forceinline__ void gather4(rsrc_t rs, const int (&a)[4], v4u (&v)[4]) {
#pragma unroll
  for (int i = 0; i < 4; ++i) v[i] = LD4(rs, a[i], 0, AUX_SC1);
#pragma unroll 1
  for (u32 spins = 0; spins < SPIN_CAP; ++spins) {
    const u32 m0 = umax(umax(v[0].x, v[0].y), umax(v[0].z, v[0].w)), m1 = umax(umax(v[1].x, v[1].y), umax(v[1].z, v[1].w));
    const u32 m2 = umax(umax(v[2].x, v[2].y), umax(v[2].z, v[2].w)), m3 = umax(umax(v[3].x, v[3].y), umax(v[3].z, v[3].w));
    if (__builtin_expect(__builtin_amdgcn_ballot_w64(umax(umax(m0, m1), umax(m2, m3)) == SENT) == 0ull, 1)) break;
    CLOB();
    if (m0 == SENT) v[0] = LD4(rs, a[0], 0, AUX_SC1);
    if (m1 == SENT) v[1] = LD4(rs, a[1], 0, AUX_SC1);
    if (m2 == SENT) v[2] = LD4(rs, a[2], 0, AUX_SC1);
    if (m3 == SENT) v[3] = LD4(rs, a[3], 0, AUX_SC1);
  }
}
__device__ __forceinline__ v2u poll2(rsrc_t rs, int a) {
  v2u v;
#pragma unroll 1
  for (u32 spins = 0; spins < SPIN_CAP; ++spins) {
    CLOB();
    v = __builtin_amdgcn_raw_buffer_load_b64(rs, a, 0, AUX_SC1);
    if (__builtin_amdgcn_ballot_w64(umax(v.x, v.y) == SENT) == 0ull) break;
  }
  CLOB();
  return v;
}

__global__ void __launch_bounds__(512, 2)
fused_decode_kernel(const u32* __restrict__ x, const char* __restrict__ kc, const char* __restrict__ vc, const char* __restrict__ wq,
                    const char* __restrict__ wo, const char* __restrict__ w1, const char* __restrict__ w2, u32* __restrict__ out, char* ws, u64 pol) {
  __shared__ __attribute__((aligned(16))) char lds[L_END];
  STAMP(0);
  if (threadIdx.x < 8) *(lds_vu32*)(lds + L_FLAG + threadIdx.x * 4) = 0u;
  bar();
  asm volatile("" :: "s"(x), "s"(kc), "s"(vc), "s"(wq), "s"(wo), "s"(w1), "s"(w2), "s"(out), "s"(ws), "s"(pol));
  const int wg = blockIdx.x, tid = threadIdx.x, lane = tid & 63;
  const int wave = __builtin_amdgcn_readfirstlane(tid >> 6);
  const int kvh = wg & 7, s = (wg >> 3) & 7, b = wg >> 6;
  const int g = lane >> 4, n = lane & 15, half = lane >> 5;
  const rsrc_t rws = mk_rsrc(ws, WS_END);
  const size_t kvbase = ((size_t)(b * HKV + kvh) * SK + (SK - WIN) + s * 512) * (DH * 2);
  const rsrc_t rk = mk_rsrc(kc + kvbase, 512 * 256), rv = mk_rsrc(vc + kvbase, 512 * 256);
  const int c0 = wave * 16 + (wg >> 3) * 4;
  const int koff = ((lane >> 2) & 3) * 256 + g * 64 + (lane & 3) * 16, voff = lane * 16;
  const int ug = kvh * 7 + s;
  const int grp = kvh * 4 + b;
  auto side = [&](int xcd, int o) { return NUMA ? ((xcd >> 2) ^ (int)pol) & 1 : (o >> 11) & 1; };

  v4u K[16], V[16];

  if (wave == 7) {
    __builtin_amdgcn_s_setprio(3);
    const int j = wg >> 3, kq = j & 3, cg = j >> 2;
    const v2u e2 = __builtin_amdgcn_raw_buffer_load_b64(rws, vzero(), WS_E, AUX_SC1);
    const rsrc_t rx = mk_rsrc(x, 4 * D * 2);
    v4u xs[4], xa[2];
#pragma unroll
    for (int i = 0; i < 4; ++i) xs[i] = LD4(rx, (n >> 2) * 1024 + ((n & 3) + g * 4) * 16, i * 256, 0);
#pragma unroll
    for (int u = 0; u < 2; ++u) xa[u] = LD4(rx, (n >> 2) * 1024 + ((n & 1) * 64 + g * 8) * 2, (kq * 128 + u * 32) * 2, 0);
    const rsrc_t rq = mk_rsrc(wq + ((size_t)kq * 128 * QD + kvh * 512 + cg * 64) * 2, 128 * QD * 2);
    v4u wr[2][8];
#pragma unroll
    for (int u = 0; u < 2; ++u)
#pragma unroll
      for (int r = 0; r < 8; ++r) wr[u][r] = LD4(rq, ((n >> 3) * 64 + g * 8) * (QD * 2) + (n & 7) * 16, (u * 32 + r) * (QD * 2), AUX_NT);
    const v4u sv = {SENT, SENT, SENT, SENT};
    const int qoff = WS_QP + ((kvh * 4 + g) * 4 + kq) * 2048 + cg * 256 + (n & 7) * 32 + (n >> 3) * 16;
    ST4(sv, rws, qoff, 0);
    { const int o = A_H1 + (grp * 8 + s) * 2048 + lane * 16, p = ph(o, side(kvh, o)); ST4(sv, rws, p, 0); ST4(sv, rws, p, 1024); }
    {
      const int o2 = A_H2 + (b * 8 + kvh) * 2048 + s * 256 + n * 16, o3 = A_ST + (grp * 8 + s) * 128 + (n & 1) * 16, o4 = A_HN + (b * 8 + kvh) * 256;
      const int p = g < 2 ? ph(o2, NUMA ? g : side(0, o2)) : g == 2 ? ph(o3, side(kvh, o3)) : ph(o4, NUMA ? n : side(0, o4));
      ST4(sv, rws, (g < 2 || (n < 2 && (g == 2 || s == 0))) ? p : OOB, 0);
    }
    {
#pragma unroll
      for (int h = 0; h < 2; ++h) { const int o = A_H3 + (b * NFF + ug) * 2048 + h * 1024 + lane * 16; ST4(sv, rws, s < 7 ? ph(o, side(h * 4, o)) : OOB, 0); }
    }
    FENCE();
    STAMP(1);
    const u64 epoch = next_epoch(((u64)e2.y << 32) | e2.x);
    const v2u ep = {(u32)epoch, (u32)(epoch >> 32)};
    float sum = 0.f;
#pragma unroll
    for (int i = 0; i < 4; ++i)
#pragma unroll
      for (int r = 0; r < 4; ++r) sum += bf_lo(xs[i][r]) + bf_hi(xs[i][r]);
    sum += dppf<QP_X1>(sum); sum += dppf<QP_X2>(sum); sum = x32_add(x16_add(sum));
    const float mean = sum * (1.f / D);
    float sq = 0.f;
#pragma unroll
    for (int i = 0; i < 4; ++i)
#pragma unroll
      for (int r = 0; r < 4; ++r) { const float a = bf_lo(xs[i][r]) - mean, c = bf_hi(xs[i][r]) - mean; sq += a * a + c * c; }
    sq += dppf<QP_X1>(sq); sq += dppf<QP_X2>(sq); sq = x32_add(x16_add(sq));
    const float mb = rbf(mean), rs = __builtin_amdgcn_rcpf(rbf(__builtin_amdgcn_sqrtf(rbf(rbf(sq * (1.f / D)) + 1e-5f))));
    v4u an[2];
#pragma unroll
    for (int u = 0; u < 2; ++u)
#pragma unroll
      for (int r = 0; r < 4; ++r) an[u][r] = pk_bf16(rbf(bf_lo(xa[u][r]) - mb) * rs, rbf(bf_hi(xa[u][r]) - mb) * rs);
    FENCE();
    asm volatile("s_waitcnt vmcnt(0)" ::: "memory");
    if (lane == 0) __builtin_amdgcn_raw_buffer_store_b64(ep, rws, 0, WS_FLAG + wg * 8, AUX_SC1);
    STAMP(2);
    v4f qa[8];
#pragma unroll
    for (int t = 0; t < 8; ++t) qa[t] = v4f{0.f, 0.f, 0.f, 0.f};
#pragma unroll
    for (int u = 0; u < 2; ++u) {
      v4u f[8]; tr8(wr[u], f);
#pragma unroll
      for (int t = 0; t < 8; ++t) qa[t] = mfma32(an[u], f[t], qa[t]);
    }
    v4u qo;
#pragma unroll
    for (int i = 0; i < 4; ++i) { const float lo = qa[i][0] + dppf<ROW_ROR8>(qa[i][1]), hi = qa[4 + i][1] + dppf<ROW_ROR8>(qa[4 + i][0]); qo[i] = san(n < 8 ? lo : hi); }
    ST4(qo, rws, qoff, 0);
    STAMP(3);
    pause<QDLY>();
#pragma unroll 1
    for (u32 spins = 0; spins < SPIN_CAP; ++spins) {
      CLOB();
      const v4u f0 = LD4(rws, lane * 16, WS_FLAG, AUX_SC1), f1 = LD4(rws, lane * 16, WS_FLAG + 1024, AUX_SC1);
      const bool bad = ((f0.x ^ ep.x) | (f0.y ^ ep.y) | (f0.z ^ ep.x) | (f0.w ^ ep.y) | (f1.x ^ ep.x) | (f1.y ^ ep.y) | (f1.z ^ ep.x) | (f1.w ^ ep.y)) != 0u;
      if (__builtin_amdgcn_ballot_w64(bad) == 0ull) break;
    }
    CLOB();
    STAMP(14);
    if (wg == 0 && lane == 0) __builtin_amdgcn_raw_buffer_store_b64(ep, rws, 0, WS_E, AUX_SC1);
    v4u qp[2][4];
#pragma unroll 1
    for (u32 spins = 0; spins < SPIN_CAP; ++spins) {
      CLOB();
      bool bad = false;
#pragma unroll
      for (int i = 0; i < 2; ++i)
#pragma unroll
        for (int k = 0; k < 4; ++k) qp[i][k] = LD4(rws, lane * 16, WS_QP + (grp * 4 + k) * 2048 + i * 1024, AUX_SC1);
#pragma unroll
      for (int i = 0; i < 2; ++i)
#pragma unroll
        for (int k = 0; k < 4; ++k) bad |= unset(qp[i][k]);
      if (__builtin_amdgcn_ballot_w64(bad) == 0ull) break;
    }
#pragma unroll
    for (int i = 0; i < 2; ++i) {
      float q4[4];
#pragma unroll
      for (int r = 0; r < 4; ++r) q4[r] = (f_of(qp[i][0][r]) + f_of(qp[i][1][r])) + (f_of(qp[i][2][r]) + f_of(qp[i][3][r]));
      *(v2u*)(lds + L_Q + i * 512 + lane * 8) = v2u{pk_bf16(q4[0], q4[1]), pk_bf16(q4[2], q4[3])};
    }
    asm volatile("s_waitcnt lgkmcnt(0)" ::: "memory");
    if (lane == 0) *(lds_vu32*)(lds + L_FLAG + 16) = 1u;
    __builtin_amdgcn_s_setprio(0);
    STAMP(11);
  }
  if (wave < 4) {
#pragma unroll
    for (int i = 0; i < 32; ++i) {
      if (i == PRE_K) { STAMP(1); asm volatile("s_waitcnt vmcnt(%0)" :: "i"(PRE_K - 1) : "memory"); STAMP(2); }
      if (i == 16) STAMP(3);
      if (i > WTHR) asm volatile("s_waitcnt vmcnt(%0)" :: "i"(WTHR - 1) : "memory");
      if (i < 16) K[i] = LD4(rk, koff, ((c0 + i) & 127) * 1024, AUX_NT);
      else V[i - 16] = LD4(rv, voff, ((c0 + i - 16) & 127) * 1024, AUX_NT);
      FENCE();
    }
    *(lds_vu32*)(lds + L_FLAG + wave * 4) = 1u;
  } else {
    lds_wait(lds + L_FLAG + (wave - 4) * 4);
#pragma unroll
    for (int i = 0; i < 16; ++i) K[i] = LD4(rk, koff, ((c0 + i) & 127) * 1024, AUX_NT);
#pragma unroll
    for (int i = 0; i < 16; ++i) V[i] = LD4(rv, voff, ((c0 + i) & 127) * 1024, AUX_NT);
  }
  FENCE();
  STAMP(4);

  lds_wait(lds + L_FLAG + 16);
  STAMP(5);

  float E[4], mloc;
  {
    const v4u qb = *(const v4u*)(lds + L_Q + (n >> 2) * 256 + g * 64 + (n & 3) * 16);
    v4f d[16];
#pragma unroll
    for (int i = 0; i < 16; ++i) d[i] = mfma32(K[i], qb, v4f{0.f, 0.f, 0.f, 0.f});
    FENCE();
    float t[16];
#pragma unroll
    for (int i = 0; i < 16; ++i) t[i] = (i & 3) == 0 ? d[i][0] + dppf<0x55>(d[i][1]) : d[i][i & 3] + dppf<0x00>(d[i][0]);
    FENCE();
#pragma unroll
    for (int i = 0; i < 16; ++i) t[i] += (i & 3) < 2 ? dppf<0xAA>(d[i][2]) : dppf<0x55>(d[i][1]);
    FENCE();
#pragma unroll
    for (int i = 0; i < 16; ++i) t[i] += (i & 3) < 3 ? dppf<0xFF>(d[i][3]) : dppf<0xAA>(d[i][2]);
    FENCE();
    float S[4];
#pragma unroll
    for (int k = 0; k < 4; ++k) {
      const float t0 = t[4 * k], t1 = t[4 * k + 1], t2 = t[4 * k + 2], t3 = t[4 * k + 3];
      const float a = (lane & 1) ? t1 : t0, c = (lane & 1) ? t3 : t2;
      S[k] = rbf(rbf((lane & 2) ? c : a) * 0.08838834764831845f);
    }
    float m = fmaxf(fmaxf(S[0], S[1]), fmaxf(S[2], S[3]));
    m = fmaxf(m, dppf<QP_X1>(m)); m = fmaxf(m, dppf<QP_X2>(m)); m = x32_max(x16_max(m));
    float l = 0.f;
#pragma unroll
    for (int k = 0; k < 4; ++k) { E[k] = __builtin_amdgcn_exp2f((S[k] - m) * LOG2E); l += E[k]; }
    l += dppf<QP_X1>(l); l += dppf<QP_X2>(l); l = x32_add(x16_add(l));
    if (lane < 16 && (lane & 3) == 0) *(v2f*)(lds + L_ST + (lane >> 2) * 64 + wave * 8) = v2f{m, l};
    mloc = m;
  }
  auto pv = [&](float f, v4f& o0, v4f& o1, auto&& mid) {
    const u32 m0 = (lane & 3) == 0 ? 0x0000FFFFu : (lane & 3) == 1 ? 0xFFFF0000u : 0u;
    const u32 m1 = (lane & 3) == 2 ? 0x0000FFFFu : (lane & 3) == 3 ? 0xFFFF0000u : 0u;
    u32 P[4];
#pragma unroll
    for (int k = 0; k < 4; ++k) P[k] = pk_bf16(E[k] * f, E[k] * f);
    v2u A[16];
#pragma unroll
    for (int i = 0; i < 16; ++i) {
      const u32 pb = (i & 3) == 0 ? dppu<0x00>(P[i >> 2]) : (i & 3) == 1 ? dppu<0x55>(P[i >> 2]) : (i & 3) == 2 ? dppu<0xAA>(P[i >> 2]) : dppu<0xFF>(P[i >> 2]);
      A[i] = v2u{pb & m0, pb & m1};
    }
    FENCE();
    o0 = v4f{0.f, 0.f, 0.f, 0.f}; o1 = o0;
#pragma unroll
    for (int i = 0; i < 16; ++i) {
      if (i == HINT1_AT) { FENCE(); mid(); FENCE(); }
      o0 = mfma16(A[i], v2u{V[i].x, V[i].y}, o0);
      o1 = mfma16(A[i], v2u{V[i].z, V[i].w}, o1);
    }
    if (HINT1_AT >= 16) { FENCE(); mid(); FENCE(); }
  };
  auto wg_stats = [&](int h, float& M, float& L) {
    v4f q[4];
#pragma unroll
    for (int i = 0; i < 4; ++i) q[i] = *(const v4f*)(lds + L_ST + h * 64 + i * 16);
    M = fmaxf(fmaxf(fmaxf(q[0].x, q[0].z), fmaxf(q[1].x, q[1].z)), fmaxf(fmaxf(q[2].x, q[2].z), fmaxf(q[3].x, q[3].z)));
    L = 0.f;
#pragma unroll
    for (int i = 0; i < 4; ++i) { L = fmaf(__builtin_amdgcn_exp2f((q[i].x - M) * LOG2E), q[i].y, L); L = fmaf(__builtin_amdgcn_exp2f((q[i].z - M) * LOG2E), q[i].w, L); }
  };

  if (wave < 4) {
    v4f o0, o1;
    pv(1.f, o0, o1, [] {});
    STAMP(6);
    v4u fa[8];
    u32 xp = 0;
    const bool dv = (n >> 2) == g;
    const int rowb = (g & 1) * 64 + (g >> 1) * 2;
    if (s < 7) {
      const rsrc_t r1 = mk_rsrc(w1 + ((size_t)(wave * 128) * FF + ug * 32) * 2, 128 * FF * 2 - ug * 64);
      v4u wa[8];
#pragma unroll
      for (int r = 0; r < 8; ++r) wa[r] = LD4(r1, (rowb + (n >> 2) * 16) * (FF * 2) + (n & 3) * 16, ((r >> 1) * 4 + (r & 1)) * (FF * 2), AUX_NT);
      xp = x[(b * D + wave * 128 + (lane & 31) * 4 + half * 2) / 2];
      FENCE();
      tr8(wa, fa);
      const v4u ones = {0x3F803F80u, 0x3F803F80u, 0x3F803F80u, 0x3F803F80u};
      v4f cs[8];
#pragma unroll
      for (int t = 0; t < 8; ++t) cs[t] = mfma32(ones, fa[t], v4f{0.f, 0.f, 0.f, 0.f});
      if (dv) {
        float* p = (float*)(lds + L_CS + (wave * 4 + g) * 128 + (n & 3) * 32);
#pragma unroll
        for (int t = 0; t < 8; ++t) p[t] = cs[t][0];
      }
    } else {
#pragma unroll
      for (int t = 0; t < 8; ++t) fa[t] = v4u{0u, 0u, 0u, 0u};
    }
    int a1[4], a2[4];
    const int cpos = (lane & 24) | ((lane + 2 * b) & 7);
#pragma unroll
    for (int i = 0; i < 4; ++i) {
      const int o = A_H1 + (grp * 8 + 2 * i + half) * 2048 + wave * 512 + cpos * 16; a1[i] = ph(o, side(kvh, o));
      const int o2 = A_H2 + (b * 8 + 2 * i + half) * 2048 + wave * 512 + (lane & 31) * 16; a2[i] = ph(o2, side(kvh, o2));
    }
    const int ost = A_ST + (grp * 8 + (lane & 7)) * 128 + wave * 8; int ast = ph(ost, side(kvh, ost));
    const int ohn = A_HN + (b * 8 + (lane & 7)) * 256; int ahn = ph(ohn, side(kvh, ohn));
    const int ohw = A_HN + (b * 8 + kvh) * 256; int ahw = (wave == 0 && s == 0 && lane < (NUMA ? 2 : 1)) ? ph(ohw, NUMA ? lane : side(0, ohw)) : OOB;
    const int o2w = A_H2 + (b * 8 + kvh) * 2048 + s * 256 + n * 16; int a2w = lane < (NUMA ? 32 : 16) ? ph(o2w, NUMA ? g : side(0, o2w)) : OOB;
    const int col = (kvh * 4 + (s & 3)) * 16;
    int a3[4];
#pragma unroll
    for (int i = 0; i < 4; ++i) { const int o = A_H3 + (b * NFF + i * 14 + (lane >> 2)) * 2048 + col * 4 + (lane & 3) * 16; a3[i] = lane < 56 ? ph(o, side(kvh, o)) : OOB; }
    int aout = n < 4 ? (b * D + col + n * 4 + g) * 2 : OOB;
    const rsrc_t rout = mk_rsrc(out, 4 * D * 2);
#pragma unroll
    for (int i = 0; i < 4; ++i) asm volatile("" : "+v"(a1[i]), "+v"(a2[i]), "+v"(a3[i]));
    asm volatile("" : "+v"(ast), "+v"(ahn), "+v"(ahw), "+v"(a2w), "+v"(aout));
    STAMP(7);
    bar();
    {
      float M, L;
      wg_stats(g, M, L);
      const float f = __builtin_amdgcn_exp2f((*(const float*)(lds + L_ST + g * 64 + wave * 8) - M) * LOG2E);
      *(v4f*)(lds + L_ACC + wave * 2048 + lane * 32) = o0 * f;
      *(v4f*)(lds + L_ACC + wave * 2048 + lane * 32 + 16) = o1 * f;
    }
    FENCE();
    const rsrc_t ro = mk_rsrc(wo + ((size_t)(kvh * 512 + wave * 128) * D + s * 64) * 2, 128 * D * 2 - s * 128);
    const int wooff = (rowb + (n >> 3) * 32) * (D * 2) + (n & 7) * 16;
    v4u wr[2][8];
#pragma unroll
    for (int r = 0; r < 8; ++r) wr[0][r] = LD4(ro, wooff, ((((r >> 1) + 2 * b) & 7) * 4 + (r & 1)) * (D * 2), AUX_NT);
    bar();
#pragma unroll
    for (int r = 0; r < 8; ++r) wr[1][r] = LD4(ro, wooff, (((4 + (r >> 1) + 2 * b) & 7) * 4 + (r & 1)) * (D * 2), AUX_NT);
    FENCE();
    STAMP(8);
    v4u f0[8], f1[8];
    tr8(wr[0], f0);
#pragma unroll
    for (int t = 0; t < 8; ++t) asm volatile("" : "+v"(f0[t]));
    const v2u stt = poll2(rws, ast);
    if (HINT2_EARLY) __builtin_amdgcn_raw_buffer_store_b64(v2u{1u, 1u}, rws, ahw, 0, AUX_SC1);
    STAMP(9);
    pause<DLY1>();
    v4u part[4];
#pragma unroll
    for (int i = 0; i < 4; ++i) part[i] = LD4(rws, a1[i], 0, AUX_SC1);
    FENCE();
    tr8(wr[1], f1);
    const float ms = f_of(stt.x);
    float M = fmaxf(ms, dppf<QP_X1>(ms)); M = fmaxf(M, dppf<QP_X2>(M)); M = fmaxf(M, dppf<ROW_HMIRROR>(M));
    const float fs = __builtin_amdgcn_exp2f((ms - M) * LOG2E);
    float L = fs * f_of(stt.y); L += dppf<QP_X1>(L); L += dppf<QP_X2>(L); L += dppf<ROW_HMIRROR>(L);
    const float wsn = fs * __builtin_amdgcn_rcpf(L);
    const float wsx = dppf<QP_X1>(wsn), wsh = half ? wsx : wsn;
    float wt[4] = {dppf<0x150>(wsh), dppf<0x152>(wsh), dppf<0x154>(wsh), dppf<0x156>(wsh)};
#pragma unroll
    for (int t = 0; t < 8; ++t) asm volatile("" : "+v"(f1[t]));
    asm volatile("" : "+v"(wt[0]), "+v"(wt[1]), "+v"(wt[2]), "+v"(wt[3]));
    FENCE();
    if (__builtin_expect(__builtin_amdgcn_ballot_w64(umax(umax(vmax(part[0]), vmax(part[1])), umax(vmax(part[2]), vmax(part[3]))) == SENT) != 0ull, 0)) gather4(rws, a1, part);
    if (!HINT2_EARLY) __builtin_amdgcn_raw_buffer_store_b64(v2u{1u, 1u}, rws, ahw, 0, AUX_SC1);
    STAMP(10);
    {
      v4f o = __builtin_bit_cast(v4f, part[0]) * wt[0];
#pragma unroll
      for (int i = 1; i < 4; ++i) o += __builtin_bit_cast(v4f, part[i]) * wt[i];
      const u32 op = pk_bf16(t32_add(o.x, o.z), t32_add(o.y, o.w));
      const v4u a0 = {op, dppu<ROW_SHL(1)>(op), dppu<ROW_SHL(2)>(op), dppu<ROW_SHL(3)>(op)};
      const v4u a1v = {dppu<ROW_SHL(4)>(op), dppu<ROW_SHL(5)>(op), dppu<ROW_SHL(6)>(op), dppu<ROW_SHL(7)>(op)};
      v4f dd[8];
#pragma unroll
      for (int t = 0; t < 8; ++t) dd[t] = mfma32(a1v, f1[t], mfma32(a0, f0[t], v4f{0.f, 0.f, 0.f, 0.f}));
      if (lane < 8 || (lane >= 40 && lane < 48)) {
        float* p = (float*)(lds + L_RB + (wave * 2 + half) * 256 + (lane & 7) * 32);
#pragma unroll
        for (int t = 0; t < 8; ++t) p[t] = dd[t][0];
      }
    }
    bar();
    if (wave == 0) {
      const v4f r0 = *(const v4f*)(lds + L_RB + (g * 2) * 256 + n * 16), r1 = *(const v4f*)(lds + L_RB + (g * 2 + 1) * 256 + n * 16);
      v4u o;
#pragma unroll
      for (int r = 0; r < 4; ++r) o[r] = san(x32_add(x16_add(r0[r] + r1[r])));
      ST4(o, rws, a2w, 0);
    }
    STAMP(11);
    if (s < 7) {
      poll2(rws, ahn);
      pause<DLY2>();
      gather4(rws, a2, part);
      STAMP(12);
      const v4f sm = (__builtin_bit_cast(v4f, part[0]) + __builtin_bit_cast(v4f, part[1])) + (__builtin_bit_cast(v4f, part[2]) + __builtin_bit_cast(v4f, part[3]));
      const u32 at = pk_bf16(t32_add(sm.x, sm.z), t32_add(sm.y, sm.w));
      const u32 hp = pk_bf16(bf_lo(xp) + bf_lo(at), bf_hi(xp) + bf_hi(at));
      const v4u a = {hp, dppu<ROW_SHL(1)>(hp), dppu<ROW_SHL(2)>(hp), dppu<ROW_SHL(3)>(hp)};
      v4f dd[8];
#pragma unroll
      for (int t = 0; t < 8; ++t) dd[t] = mfma32(a, fa[t], v4f{0.f, 0.f, 0.f, 0.f});
      *(u32*)(lds + L_H + wave * 256 + (lane & 31) * 8 + half * 4) = hp;
      const float cs0 = bf_lo((u32)__builtin_amdgcn_readfirstlane((int)hp));
      const float d0 = bf_lo(hp) - cs0, d1 = bf_hi(hp) - cs0;
      const float s12 = x16_add(row16_sum(t32_add(d0 + d1, d0 * d0 + d1 * d1)));
      if ((lane & 31) == 0) *(v2f*)(lds + L_LN + wave * 16 + half * 8) = v2f{s12, cs0};
      if (dv) {
        float* p = (float*)(lds + L_RC + (wave * 4 + g) * 128 + (n & 3) * 32);
#pragma unroll
        for (int t = 0; t < 8; ++t) p[t] = dd[t][0];
      }
      bar();
      STAMP(13);
      if (s < 4 && wave == 0) {
        const v2u hq = *(const v2u*)(lds + L_H + (col + (lane & 3) * 4) * 2);
        pause<DLY3>();
        gather4(rws, a3, part);
        STAMP(14);
        v4f r = (__builtin_bit_cast(v4f, part[0]) + __builtin_bit_cast(v4f, part[1])) + (__builtin_bit_cast(v4f, part[2]) + __builtin_bit_cast(v4f, part[3]));
        float y = t16_add(t32_add(r.x, r.z), t32_add(r.y, r.w));
        y += dppf<ROW_ROR4>(y); y += dppf<ROW_ROR8>(y);
        const u32 hw = half ? hq.y : hq.x;
        const float hv = (g & 1) ? bf_hi(hw) : bf_lo(hw);
        const u32 ob = pk_bf16(hv + rbf(y), 0.f);
        __builtin_amdgcn_raw_buffer_store_b16((unsigned short)ob, rout, aout, 0, 0);
      }
    }
  } else {
    const int w4 = wave - 4;
    v4u wb[8], fb[8];
    const rsrc_t r2 = mk_rsrc(w2 + ((size_t)(ug * 32) * D + w4 * 128) * 2, s < 7 ? 32 * D * 2 - w4 * 256 : 0);
    STAMP(6);
    bar();
    STAMP(7);
    float M, L;
    wg_stats(n >> 2, M, L);
    const int ost = A_ST + (grp * 8 + s) * 128 + (n >> 2) * 8;
    const int ast = (wave == 7 && lane < 16 && (lane & 3) == 0) ? ph(ost, side(kvh, ost)) : OOB;
    const int o1w = A_H1 + (grp * 8 + s) * 2048 + w4 * 512 + (lane & 31) * 16, a1t = ph(o1w, side(kvh, o1w));
    int a1w = lane < 32 ? a1t : OOB;
    asm volatile("" : "+v"(a1w));
    const v2u stv = {san(M), san(L)};
    v4f o0, o1;
    pv(__builtin_amdgcn_exp2f((mloc - M) * LOG2E), o0, o1, [&] {
      __builtin_amdgcn_raw_buffer_store_b64(stv, rws, ast, 0, AUX_SC1);
    });
    *(v4f*)(lds + L_ACC + wave * 2048 + lane * 32) = o0;
    *(v4f*)(lds + L_ACC + wave * 2048 + lane * 32 + 16) = o1;
    bar();
    STAMP(8);
    {
      v4f a[4];
#pragma unroll
      for (int i = 0; i < 4; ++i) a[i] = *(const v4f*)(lds + L_ACC + (half * 4 + i) * 2048 + w4 * 512 + (lane & 31) * 16);
      const v4f o = (a[0] + a[1]) + (a[2] + a[3]);
      v4u ou;
#pragma unroll
      for (int r = 0; r < 4; ++r) ou[r] = san(x32_add(o[r]));
      ST4(ou, rws, a1w, 0);
    }
    STAMP(9);
    if (W2_AT == 1) {
#pragma unroll
      for (int r = 0; r < 8; ++r) wb[r] = LD4(r2, (g * 8) * (D * 2) + n * 16, r * (D * 2), AUX_NT);
    }
    bar();
    if (s < 7) {
      if (W2_AT == 2) {
#pragma unroll
        for (int r = 0; r < 8; ++r) wb[r] = LD4(r2, (g * 8) * (D * 2) + n * 16, r * (D * 2), AUX_NT);
      }
      tr8(wb, fb);
      float csum;
      {
        float pp[8];
#pragma unroll
        for (int i = 0; i < 8; ++i) pp[i] = *(const float*)(lds + L_CS + ((lane & 8) + i) * 128 + (g * 8 + (lane & 7)) * 4);
        csum = ((pp[0] + pp[1]) + (pp[2] + pp[3])) + ((pp[4] + pp[5]) + (pp[6] + pp[7]));
        csum += dppf<ROW_ROR8>(csum);
      }
      int a3w[2];
#pragma unroll
      for (int h = 0; h < 2; ++h) { const int o = A_H3 + (b * NFF + ug) * 2048 + w4 * 512 + n * 32 + h * 16; a3w[h] = lane < 16 ? ph(o, side(w4 * 2, o)) : OOB; }
#pragma unroll
      for (int t = 0; t < 8; ++t) asm volatile("" : "+v"(fb[t]));
      asm volatile("" : "+v"(csum), "+v"(a3w[0]), "+v"(a3w[1]));
      STAMP(10);
      bar();
      STAMP(12);
      float pp[8];
#pragma unroll
      for (int i = 0; i < 8; ++i) pp[i] = *(const float*)(lds + L_RC + ((lane & 8) + i) * 128 + (g * 8 + (lane & 7)) * 4);
      const v4f ln = *(const v4f*)(lds + L_LN + (lane & 3) * 16);
      float mean = fmaf(ln.x, 1.f / D, ln.y * 0.25f);
      mean += dppf<QP_X1>(mean); mean += dppf<QP_X2>(mean);
      const float dm = mean - ln.y;
      float var = fmaf(dm, fmaf(128.f, dm, -2.f * ln.x), ln.z);
      var += dppf<QP_X1>(var); var += dppf<QP_X2>(var);
      const float mb = rbf(mean), rs = __builtin_amdgcn_rcpf(rbf(__builtin_amdgcn_sqrtf(rbf(rbf(fmaxf(var, 0.f) * (1.f / D)) + 1e-5f))));
      float ps = ((pp[0] + pp[1]) + (pp[2] + pp[3])) + ((pp[4] + pp[5]) + (pp[6] + pp[7]));
      ps += dppf<ROW_ROR8>(ps);
      const float pre = rbf(fmaf(-mb, csum, ps) * rs);
      const float h1 = pre * __builtin_amdgcn_rcpf(1.f + __builtin_amdgcn_exp2f(-pre * LOG2E));
      const u32 hq = pk_bf16(h1, dppf<ROW_SHL(1)>(h1));
      const v4u a2 = {hq, dppu<ROW_SHL(2)>(hq), dppu<ROW_SHL(4)>(hq), dppu<ROW_SHL(6)>(hq)};
      v4f dd[8];
#pragma unroll
      for (int t = 0; t < 8; ++t) dd[t] = mfma32(a2, fb[t], v4f{0.f, 0.f, 0.f, 0.f});
      ST4((v4u{san(dd[0][0]), san(dd[1][0]), san(dd[2][0]), san(dd[3][0])}), rws, a3w[0], 0);
      ST4((v4u{san(dd[4][0]), san(dd[5][0]), san(dd[6][0]), san(dd[7][0])}), rws, a3w[1], 0);
      STAMP(13);
    }
  }
}

__global__ void __launch_bounds__(512, 2) k_x() { asm volatile("v_mov_b32 v165, 0" ::: "v165"); }
constexpr int g_pol = 0;
extern "C" void kernel_launch(void* const* d_in, const int* in_sizes, int n_in, void* d_out, int out_size, void* d_ws, size_t ws_size,
                              hipStream_t stream) {
  fused_decode_kernel<<<NWG, 512, 0, stream>>>((const u32*)d_in[0], (const char*)d_in[1], (const char*)d_in[2], (const char*)d_in[3],
                                               (const char*)d_in[4], (const char*)d_in[6], (const char*)d_in[7], (u32*)d_out, (char*)d_ws, (u64)g_pol);
  k_x<<<256,512,25600,stream>>>();
}
```

```cpp
#define DLY3 20
#define W2_AT 2
#include <hip/hip_runtime.h>
#include <hip/hip_bf16.h>
#include <stdint.h>

typedef unsigned u32;
typedef unsigned long long u64;
typedef u32 v4u __attribute__((ext_vector_type(4)));
typedef u32 v2u __attribute__((ext_vector_type(2)));
typedef float v4f __attribute__((ext_vector_type(4)));
typedef float v2f __attribute__((ext_vector_type(2)));
typedef __bf16 bf16x8 __attribute__((ext_vector_type(8)));
typedef __bf16 bf16x2 __attribute__((ext_vector_type(2)));
typedef short s16x4 __attribute__((ext_vector_type(4)));
typedef __amdgpu_buffer_rsrc_t rsrc_t;

constexpr int D = 512, HKV = 8, SK = 16384, DH = 128, WIN = 4096, FF = 1792, QD = 4096;
constexpr int NWG = 256, NFF = 56;
constexpr u32 SENT = 0xFFFFFFFFu;
constexpr int AUX_NT = 2, AUX_SC1 = 16;
#define NUMA 0
#define HINT1_AT 14
#define HINT2_EARLY 1
#define DLY1 0
#define DLY2 0
#define PRE_K 4
#define WTHR 6
#define QDLY 20
#define H1_SPLIT 8
constexpr int NST = 6;
constexpr int OOB = 0x7FFFFF00;
constexpr u32 SPIN_CAP = 1u << 20;
constexpr float LOG2E = 1.4426950408889634f;

constexpr int A_H1 = 0;
constexpr int A_H2 = A_H1 + NWG * 2048;
constexpr int A_ST = A_H2 + 32 * 2048;
constexpr int A_HN = A_ST + NWG * 128;
constexpr int A_H3 = A_HN + 32 * 256;
constexpr int A_END = A_H3 + 4 * NFF * 2048;
constexpr int WS_E = 2 * ((A_END + 16383) / 16384) * 16384;
constexpr int WS_FLAG = WS_E + 4096;
constexpr int WS_QP = WS_FLAG + 4096;
constexpr int WS_END = WS_QP + 8 * 4 * 4 * 2048;
constexpr int WS_DBG = 8 << 20;
__device__ __forceinline__ int ph(int o, int side) { const int p = ((o >> 13) << 14) | (o & 8191); return p | (((side ^ (p >> 21)) & 1) << 13); }

constexpr int L_Q = 0;
constexpr int L_FLAG = 1024;
constexpr int L_ST = 1088;
constexpr int L_ACC = 2048;
constexpr int L_RB = L_ACC + 16384;
constexpr int L_RC = L_RB + 2048;
constexpr int L_CS = L_RC + 2048 + 64;
constexpr int L_LN = L_CS + 2048 + 64;
constexpr int L_H = L_LN + 64;
constexpr int L_END = L_H + 1024;

#define STAMP(i) do {} while (0)

__device__ __forceinline__ rsrc_t mk_rsrc(const void* p, u32 bytes) { return __builtin_amdgcn_make_buffer_rsrc((void*)p, 0, bytes, 0x00020000); }
__device__ __forceinline__ float f_of(u32 x) { return __builtin_bit_cast(float, x); }
__device__ __forceinline__ u32 u_of(float x) { return __builtin_bit_cast(u32, x); }
__device__ __forceinline__ u32 pk_bf16(float lo, float hi) { v2f t = {lo, hi}; return __builtin_bit_cast(u32, __builtin_convertvector(t, bf16x2)); }
__device__ __forceinline__ float bf_lo(u32 x) { return f_of(x << 16); }
__device__ __forceinline__ float bf_hi(u32 x) { return f_of(x & 0xFFFF0000u); }
__device__ __forceinline__ float rbf(float x) { return bf_lo(pk_bf16(x, x)); }
__device__ __forceinline__ u32 san(float x) { u32 u = u_of(x); return u < SENT ? u : SENT - 1u; }
__device__ __forceinline__ u32 umax(u32 a, u32 b) { return a > b ? a : b; }
__device__ __forceinline__ u32 vmax(v4u v) { return umax(umax(v.x, v.y), umax(v.z, v.w)); }
__device__ __forceinline__ bool unset(v4u v) { return umax(umax(v.x, v.y), umax(v.z, v.w)) == SENT; }
template <int CTRL> __device__ __forceinline__ u32 dppu(u32 x) { return (u32)__builtin_amdgcn_mov_dpp((int)x, CTRL, 0xf, 0xf, true); }
template <int CTRL> __device__ __forceinline__ float dppf(float x) { return f_of(dppu<CTRL>(u_of(x))); }
constexpr int QP_X1 = 0xB1, QP_X2 = 0x4E, ROW_ROR4 = 0x124, ROW_ROR8 = 0x128, ROW_HMIRROR = 0x141;
constexpr int ROW_SHL(int n) { return 0x100 + n; }
__device__ __forceinline__ float x16_add(float x) { auto s = __builtin_amdgcn_permlane16_swap(u_of(x), u_of(x), false, false); return f_of(s[0]) + f_of(s[1]); }
__device__ __forceinline__ float x32_add(float x) { auto s = __builtin_amdgcn_permlane32_swap(u_of(x), u_of(x), false, false); return f_of(s[0]) + f_of(s[1]); }
__device__ __forceinline__ float x16_max(float x) { auto s = __builtin_amdgcn_permlane16_swap(u_of(x), u_of(x), false, false); return fmaxf(f_of(s[0]), f_of(s[1])); }
__device__ __forceinline__ float x32_max(float x) { auto s = __builtin_amdgcn_permlane32_swap(u_of(x), u_of(x), false, false); return fmaxf(f_of(s[0]), f_of(s[1])); }
__device__ __forceinline__ float t32_add(float a, float b) { auto s = __builtin_amdgcn_permlane32_swap(u_of(a), u_of(b), false, false); return f_of(s[0]) + f_of(s[1]); }
__device__ __forceinline__ float t16_add(float a, float b) { auto s = __builtin_amdgcn_permlane16_swap(u_of(a), u_of(b), false, false); return f_of(s[0]) + f_of(s[1]); }
__device__ __forceinline__ float row16_sum(float x) { x += dppf<QP_X1>(x); x += dppf<QP_X2>(x); x += dppf<ROW_ROR4>(x); x += dppf<ROW_ROR8>(x); return x; }
__device__ __forceinline__ v4f mfma32(v4u a, v4u b, v4f c) { return __builtin_amdgcn_mfma_f32_16x16x32_bf16(__builtin_bit_cast(bf16x8, a), __builtin_bit_cast(bf16x8, b), c, 0, 0, 0); }
__device__ __forceinline__ v4f mfma16(v2u a, v2u b, v4f c) { return __builtin_amdgcn_mfma_f32_16x16x16bf16_1k(__builtin_bit_cast(s16x4, a), __builtin_bit_cast(s16x4, b), c, 0, 0, 0); }
#define FENCE() __builtin_amdgcn_sched_barrier(0)
#define CLOB() asm volatile("" ::: "memory")
__device__ __forceinline__ void bar() { FENCE(); asm volatile("s_waitcnt lgkmcnt(0)\n\ts_barrier" ::: "memory"); FENCE(); }
typedef volatile __attribute__((address_space(3))) u32 lds_vu32;
__device__ __forceinline__ int vzero() { int z; asm volatile("v_mov_b32 %0, 0" : "=v"(z)); return z; }
template <int N> __device__ __forceinline__ void pause() { if (N > 0) { FENCE(); __builtin_amdgcn_s_sleep(N); FENCE(); } }
#define LD4(r, v, s, aux) __builtin_amdgcn_raw_buffer_load_b128(r, v, s, aux)
#define ST4(d, r, v, s) __builtin_amdgcn_raw_buffer_store_b128(d, r, v, s, AUX_SC1)

__device__ __forceinline__ void tr8(const v4u (&w)[8], v4u (&f)[8]) {
#pragma unroll
  for (int t = 0; t < 8; ++t)
#pragma unroll
    for (int i = 0; i < 4; ++i) f[t][i] = __builtin_amdgcn_perm(w[2 * i + 1][t >> 1], w[2 * i][t >> 1], (t & 1) ? 0x07060302u : 0x05040100u);
}

__device__ __forceinline__ u64 next_epoch(u64 e) {
  u64 z = e * 0x9E3779B97F4A7C15ull + 0xD1B54A32D192ED03ull;
  z ^= z >> 29; z *= 0xBF58476D1CE4E5B9ull; z ^= z >> 32;
  return (z & ~1ull) | (~e & 1ull);
}

__device__ __forceinline__ void lds_wait(const char* p) {
#pragma unroll 1
  for (u32 spins = 0; spins < SPIN_CAP * 8u; ++spins) {
    if (__builtin_amdgcn_readfirstlane((int)*(lds_vu32*)p) != 0) break;
    __builtin_amdgcn_s_sleep(1);
  }
  CLOB();
}

__device__ __forceinline__ void gather4(rsrc_t rs, const int (&a)[4], v4u (&v)[4]) {
#pragma unroll
  for (int i = 0; i < 4; ++i) v[i] = LD4(rs, a[i], 0, AUX_SC1);
#pragma unroll 1
  for (u32 spins = 0; spins < SPIN_CAP; ++spins) {
    const u32 m0 = umax(umax(v[0].x, v[0].y), umax(v[0].z, v[0].w)), m1 = umax(umax(v[1].x, v[1].y), umax(v[1].z, v[1].w));
    const u32 m2 = umax(umax(v[2].x, v[2].y), umax(v[2].z, v[2].w)), m3 = umax(umax(v[3].x, v[3].y), umax(v[3].z, v[3].w));
    if (__builtin_expect(__builtin_amdgcn_ballot_w64(umax(umax(m0, m1), umax(m2, m3)) == SENT) == 0ull, 1)) break;
    CLOB();
    if (m0 == SENT) v[0] = LD4(rs, a[0], 0, AUX_SC1);
    if (m1 == SENT) v[1] = LD4(rs, a[1], 0, AUX_SC1);
    if (m2 == SENT) v[2] = LD4(rs, a[2], 0, AUX_SC1);
    if (m3 == SENT) v[3] = LD4(rs, a[3], 0, AUX_SC1);
  }
}
__device__ __forceinline__ v2u poll2(rsrc_t rs, int a) {
  v2u v;
#pragma unroll 1
  for (u32 spins = 0; spins < SPIN_CAP; ++spins) {
    CLOB();
    v = __builtin_amdgcn_raw_buffer_load_b64(rs, a, 0, AUX_SC1);
    if (__builtin_amdgcn_ballot_w64(umax(v.x, v.y) == SENT) == 0ull) break;
  }
  CLOB();
  return v;
}

__global__ void __launch_bounds__(512, 2)
fused_decode_kernel(const u32* __restrict__ x, const char* __restrict__ kc, const char* __restrict__ vc, const char* __restrict__ wq,
                    const char* __restrict__ wo, const char* __restrict__ w1, const char* __restrict__ w2, u32* __restrict__ out, char* ws, u64 pol) {
  __shared__ __attribute__((aligned(16))) char lds[L_END];
  STAMP(0);
  if (threadIdx.x < 8) *(lds_vu32*)(lds + L_FLAG + threadIdx.x * 4) = 0u;
  bar();
  asm volatile("" :: "s"(x), "s"(kc), "s"(vc), "s"(wq), "s"(wo), "s"(w1), "s"(w2), "s"(out), "s"(ws), "s"(pol));
  const int wg = blockIdx.x, tid = threadIdx.x, lane = tid & 63;
  const int wave = __builtin_amdgcn_readfirstlane(tid >> 6);
  const int kvh = wg & 7, s = (wg >> 3) & 7, b = wg >> 6;
  const int g = lane >> 4, n = lane & 15, half = lane >> 5;
  const rsrc_t rws = mk_rsrc(ws, WS_END);
  const size_t kvbase = ((size_t)(b * HKV + kvh) * SK + (SK - WIN) + s * 512) * (DH * 2);
  const rsrc_t rk = mk_rsrc(kc + kvbase, 512 * 256), rv = mk_rsrc(vc + kvbase, 512 * 256);
  const int c0 = wave * 16 + (wg >> 3) * 4;
  const int koff = ((lane >> 2) & 3) * 256 + g * 64 + (lane & 3) * 16, voff = lane * 16;
  const int ug = kvh * 7 + s;
  const int grp = kvh * 4 + b;
  auto side = [&](int xcd, int o) { return NUMA ? ((xcd >> 2) ^ (int)pol) & 1 : (o >> 11) & 1; };

  v4u K[16], V[16];

  if (wave == 7) {
    __builtin_amdgcn_s_setprio(3);
    const int j = wg >> 3, kq = j & 3, cg = j >> 2;
    const v2u e2 = __builtin_amdgcn_raw_buffer_load_b64(rws, vzero(), WS_E, AUX_SC1);
    const rsrc_t rx = mk_rsrc(x, 4 * D * 2);
    v4u xs[4], xa[2];
#pragma unroll
    for (int i = 0; i < 4; ++i) xs[i] = LD4(rx, (n >> 2) * 1024 + ((n & 3) + g * 4) * 16, i * 256, 0);
#pragma unroll
    for (int u = 0; u < 2; ++u) xa[u] = LD4(rx, (n >> 2) * 1024 + ((n & 1) * 64 + g * 8) * 2, (kq * 128 + u * 32) * 2, 0);
    const rsrc_t rq = mk_rsrc(wq + ((size_t)kq * 128 * QD + kvh * 512 + cg * 64) * 2, 128 * QD * 2);
    v4u wr[2][8];
#pragma unroll
    for (int u = 0; u < 2; ++u)
#pragma unroll
      for (int r = 0; r < 8; ++r) wr[u][r] = LD4(rq, ((n >> 3) * 64 + g * 8) * (QD * 2) + (n & 7) * 16, (u * 32 + r) * (QD * 2), AUX_NT);
    const v4u sv = {SENT, SENT, SENT, SENT};
    const int qoff = WS_QP + ((kvh * 4 + g) * 4 + kq) * 2048 + cg * 256 + (n & 7) * 32 + (n >> 3) * 16;
    ST4(sv, rws, qoff, 0);
    { const int o = A_H1 + (grp * 8 + s) * 2048 + lane * 16, p = ph(o, side(kvh, o)); ST4(sv, rws, p, 0); ST4(sv, rws, p, 1024); }
    {
      const int o2 = A_H2 + (b * 8 + kvh) * 2048 + s * 256 + n * 16, o3 = A_ST + (grp * 8 + s) * 128 + (n & 1) * 16, o4 = A_HN + (b * 8 + kvh) * 256;
      const int p = g < 2 ? ph(o2, NUMA ? g : side(0, o2)) : g == 2 ? ph(o3, side(kvh, o3)) : ph(o4, NUMA ? n : side(0, o4));
      ST4(sv, rws, (g < 2 || (n < 2 && (g == 2 || s == 0))) ? p : OOB, 0);
    }
    {
#pragma unroll
      for (int h = 0; h < 2; ++h) { const int o = A_H3 + (b * NFF + ug) * 2048 + h * 1024 + lane * 16; ST4(sv, rws, s < 7 ? ph(o, side(h * 4, o)) : OOB, 0); }
    }
    FENCE();
    STAMP(1);
    const u64 epoch = next_epoch(((u64)e2.y << 32) | e2.x);
    const v2u ep = {(u32)epoch, (u32)(epoch >> 32)};
    float sum = 0.f;
#pragma unroll
    for (int i = 0; i < 4; ++i)
#pragma unroll
      for (int r = 0; r < 4; ++r) sum += bf_lo(xs[i][r]) + bf_hi(xs[i][r]);
    sum += dppf<QP_X1>(sum); sum += dppf<QP_X2>(sum); sum = x32_add(x16_add(sum));
    const float mean = sum * (1.f / D);
    float sq = 0.f;
#pragma unroll
    for (int i = 0; i < 4; ++i)
#pragma unroll
      for (int r = 0; r < 4; ++r) { const float a = bf_lo(xs[i][r]) - mean, c = bf_hi(xs[i][r]) - mean; sq += a * a + c * c; }
    sq += dppf<QP_X1>(sq); sq += dppf<QP_X2>(sq); sq = x32_add(x16_add(sq));
    const float mb = rbf(mean), rs = __builtin_amdgcn_rcpf(rbf(__builtin_amdgcn_sqrtf(rbf(rbf(sq * (1.f / D)) + 1e-5f))));
    v4u an[2];
#pragma unroll
    for (int u = 0; u < 2; ++u)
#pragma unroll
      for (int r = 0; r < 4; ++r) an[u][r] = pk_bf16(rbf(bf_lo(xa[u][r]) - mb) * rs, rbf(bf_hi(xa[u][r]) - mb) * rs);
    FENCE();
    asm volatile("s_waitcnt vmcnt(0)" ::: "memory");
    if (lane == 0) __builtin_amdgcn_raw_buffer_store_b64(ep, rws, 0, WS_FLAG + wg * 8, AUX_SC1);
    STAMP(2);
    v4f qa[8];
#pragma unroll
    for (int t = 0; t < 8; ++t) qa[t] = v4f{0.f, 0.f, 0.f, 0.f};
#pragma unroll
    for (int u = 0; u < 2; ++u) {
      v4u f[8]; tr8(wr[u], f);
#pragma unroll
      for (int t = 0; t < 8; ++t) qa[t] = mfma32(an[u], f[t], qa[t]);
    }
    v4u qo;
#pragma unroll
    for (int i = 0; i < 4; ++i) { const float lo = qa[i][0] + dppf<ROW_ROR8>(qa[i][1]), hi = qa[4 + i][1] + dppf<ROW_ROR8>(qa[4 + i][0]); qo[i] = san(n < 8 ? lo : hi); }
    ST4(qo, rws, qoff, 0);
    STAMP(3);
    pause<QDLY>();
#pragma unroll 1
    for (u32 spins = 0; spins < SPIN_CAP; ++spins) {
      CLOB();
      const v4u f0 = LD4(rws, lane * 16, WS_FLAG, AUX_SC1), f1 = LD4(rws, lane * 16, WS_FLAG + 1024, AUX_SC1);
      const bool bad = ((f0.x ^ ep.x) | (f0.y ^ ep.y) | (f0.z ^ ep.x) | (f0.w ^ ep.y) | (f1.x ^ ep.x) | (f1.y ^ ep.y) | (f1.z ^ ep.x) | (f1.w ^ ep.y)) != 0u;
      if (__builtin_amdgcn_ballot_w64(bad) == 0ull) break;
    }
    CLOB();
    STAMP(14);
    if (wg == 0 && lane == 0) __builtin_amdgcn_raw_buffer_store_b64(ep, rws, 0, WS_E, AUX_SC1);
    v4u qp[2][4];
#pragma unroll 1
    for (u32 spins = 0; spins < SPIN_CAP; ++spins) {
      CLOB();
      bool bad = false;
#pragma unroll
      for (int i = 0; i < 2; ++i)
#pragma unroll
        for (int k = 0; k < 4; ++k) qp[i][k] = LD4(rws, lane * 16, WS_QP + (grp * 4 + k) * 2048 + i * 1024, AUX_SC1);
#pragma unroll
      for (int i = 0; i < 2; ++i)
#pragma unroll
        for (int k = 0; k < 4; ++k) bad |= unset(qp[i][k]);
      if (__builtin_amdgcn_ballot_w64(bad) == 0ull) break;
    }
#pragma unroll
    for (int i = 0; i < 2; ++i) {
      float q4[4];
#pragma unroll
      for (int r = 0; r < 4; ++r) q4[r] = (f_of(qp[i][0][r]) + f_of(qp[i][1][r])) + (f_of(qp[i][2][r]) + f_of(qp[i][3][r]));
      *(v2u*)(lds + L_Q + i * 512 + lane * 8) = v2u{pk_bf16(q4[0], q4[1]), pk_bf16(q4[2], q4[3])};
    }
    asm volatile("s_waitcnt lgkmcnt(0)" ::: "memory");
    if (lane == 0) *(lds_vu32*)(lds + L_FLAG + 16) = 1u;
    __builtin_amdgcn_s_setprio(0);
    STAMP(11);
  }
  if (wave < 4) {
#pragma unroll
    for (int i = 0; i < 32; ++i) {
      if (i == PRE_K) { STAMP(1); asm volatile("s_waitcnt vmcnt(%0)" :: "i"(PRE_K - 1) : "memory"); STAMP(2); }
      if (i == 16) STAMP(3);
      if (i > WTHR) asm volatile("s_waitcnt vmcnt(%0)" :: "i"(WTHR - 1) : "memory");
      if (i < 16) K[i] = LD4(rk, koff, ((c0 + i) & 127) * 1024, AUX_NT);
      else V[i - 16] = LD4(rv, voff, ((c0 + i - 16) & 127) * 1024, AUX_NT);
      FENCE();
    }
    *(lds_vu32*)(lds + L_FLAG + wave * 4) = 1u;
  } else {
    lds_wait(lds + L_FLAG + (wave - 4) * 4);
#pragma unroll
    for (int i = 0; i < 16; ++i) K[i] = LD4(rk, koff, ((c0 + i) & 127) * 1024, AUX_NT);
#pragma unroll
    for (int i = 0; i < 16; ++i) V[i] = LD4(rv, voff, ((c0 + i) & 127) * 1024, AUX_NT);
  }
  FENCE();
  STAMP(4);

  lds_wait(lds + L_FLAG + 16);
  STAMP(5);

  float E[4], mloc;
  {
    const v4u qb = *(const v4u*)(lds + L_Q + (n >> 2) * 256 + g * 64 + (n & 3) * 16);
    v4f d[16];
#pragma unroll
    for (int i = 0; i < 16; ++i) d[i] = mfma32(K[i], qb, v4f{0.f, 0.f, 0.f, 0.f});
    FENCE();
    float t[16];
#pragma unroll
    for (int i = 0; i < 16; ++i) t[i] = (i & 3) == 0 ? d[i][0] + dppf<0x55>(d[i][1]) : d[i][i & 3] + dppf<0x00>(d[i][0]);
    FENCE();
#pragma unroll
    for (int i = 0; i < 16; ++i) t[i] += (i & 3) < 2 ? dppf<0xAA>(d[i][2]) : dppf<0x55>(d[i][1]);
    FENCE();
#pragma unroll
    for (int i = 0; i < 16; ++i) t[i] += (i & 3) < 3 ? dppf<0xFF>(d[i][3]) : dppf<0xAA>(d[i][2]);
    FENCE();
    float S[4];
#pragma unroll
    for (int k = 0; k < 4; ++k) {
      const float t0 = t[4 * k], t1 = t[4 * k + 1], t2 = t[4 * k + 2], t3 = t[4 * k + 3];
      const float a = (lane & 1) ? t1 : t0, c = (lane & 1) ? t3 : t2;
      S[k] = rbf(rbf((lane & 2) ? c : a) * 0.08838834764831845f);
    }
    float m = fmaxf(fmaxf(S[0], S[1]), fmaxf(S[2], S[3]));
    m = fmaxf(m, dppf<QP_X1>(m)); m = fmaxf(m, dppf<QP_X2>(m)); m = x32_max(x16_max(m));
    float l = 0.f;
#pragma unroll
    for (int k = 0; k < 4; ++k) { E[k] = __builtin_amdgcn_exp2f((S[k] - m) * LOG2E); l += E[k]; }
    l += dppf<QP_X1>(l); l += dppf<QP_X2>(l); l = x32_add(x16_add(l));
    if (lane < 16 && (lane & 3) == 0) *(v2f*)(lds + L_ST + (lane >> 2) * 64 + wave * 8) = v2f{m, l};
    mloc = m;
  }
  auto pv = [&](float f, v4f& o0, v4f& o1, auto&& mid) {
    const u32 m0 = (lane & 3) == 0 ? 0x0000FFFFu : (lane & 3) == 1 ? 0xFFFF0000u : 0u;
    const u32 m1 = (lane & 3) == 2 ? 0x0000FFFFu : (lane & 3) == 3 ? 0xFFFF0000u : 0u;
    u32 P[4];
#pragma unroll
    for (int k = 0; k < 4; ++k) P[k] = pk_bf16(E[k] * f, E[k] * f);
    v2u A[16];
#pragma unroll
    for (int i = 0; i < 16; ++i) {
      const u32 pb = (i & 3) == 0 ? dppu<0x00>(P[i >> 2]) : (i & 3) == 1 ? dppu<0x55>(P[i >> 2]) : (i & 3) == 2 ? dppu<0xAA>(P[i >> 2]) : dppu<0xFF>(P[i >> 2]);
      A[i] = v2u{pb & m0, pb & m1};
    }
    FENCE();
    o0 = v4f{0.f, 0.f, 0.f, 0.f}; o1 = o0;
#pragma unroll
    for (int i = 0; i < 16; ++i) {
      if (i == HINT1_AT) { FENCE(); mid(); FENCE(); }
      o0 = mfma16(A[i], v2u{V[i].x, V[i].y}, o0);
      o1 = mfma16(A[i], v2u{V[i].z, V[i].w}, o1);
    }
    if (HINT1_AT >= 16) { FENCE(); mid(); FENCE(); }
  };
  auto wg_stats = [&](int h, float& M, float& L) {
    v4f q[4];
#pragma unroll
    for (int i = 0; i < 4; ++i) q[i] = *(const v4f*)(lds + L_ST + h * 64 + i * 16);
    M = fmaxf(fmaxf(fmaxf(q[0].x, q[0].z), fmaxf(q[1].x, q[1].z)), fmaxf(fmaxf(q[2].x, q[2].z), fmaxf(q[3].x, q[3].z)));
    L = 0.f;
#pragma unroll
    for (int i = 0; i < 4; ++i) { L = fmaf(__builtin_amdgcn_exp2f((q[i].x - M) * LOG2E), q[i].y, L); L = fmaf(__builtin_amdgcn_exp2f((q[i].z - M) * LOG2E), q[i].w, L); }
  };

  if (wave < 4) {
    v4f o0, o1;
    pv(1.f, o0, o1, [] {});
    STAMP(6);
    v4u fa[8];
    u32 xp = 0;
    const bool dv = (n >> 2) == g;
    const int rowb = (g & 1) * 64 + (g >> 1) * 2;
    if (s < 7) {
      const rsrc_t r1 = mk_rsrc(w1 + ((size_t)(wave * 128) * FF + ug * 32) * 2, 128 * FF * 2 - ug * 64);
      v4u wa[8];
#pragma unroll
      for (int r = 0; r < 8; ++r) wa[r] = LD4(r1, (rowb + (n >> 2) * 16) * (FF * 2) + (n & 3) * 16, ((r >> 1) * 4 + (r & 1)) * (FF * 2), AUX_NT);
      xp = x[(b * D + wave * 128 + (lane & 31) * 4 + half * 2) / 2];
      FENCE();
      tr8(wa, fa);
      const v4u ones = {0x3F803F80u, 0x3F803F80u, 0x3F803F80u, 0x3F803F80u};
      v4f cs[8];
#pragma unroll
      for (int t = 0; t < 8; ++t) cs[t] = mfma32(ones, fa[t], v4f{0.f, 0.f, 0.f, 0.f});
      if (dv) {
        float* p = (float*)(lds + L_CS + (wave * 4 + g) * 128 + (n & 3) * 32);
#pragma unroll
        for (int t = 0; t < 8; ++t) p[t] = cs[t][0];
      }
    } else {
#pragma unroll
      for (int t = 0; t < 8; ++t) fa[t] = v4u{0u, 0u, 0u, 0u};
    }
    int a1[4], a2[4];
    const int cpos = (lane & 24) | ((lane + 2 * b) & 7);
#pragma unroll
    for (int i = 0; i < 4; ++i) {
      const int o = A_H1 + (grp * 8 + 2 * i + half) * 2048 + wave * 512 + cpos * 16; a1[i] = ph(o, side(kvh, o));
      const int o2 = A_H2 + (b * 8 + 2 * i + half) * 2048 + wave * 512 + (lane & 31) * 16; a2[i] = ph(o2, side(kvh, o2));
    }
    const int ost = A_ST + (grp * 8 + (lane & 7)) * 128 + wave * 8; int ast = ph(ost, side(kvh, ost));
    const int ohn = A_HN + (b * 8 + (lane & 7)) * 256; int ahn = ph(ohn, side(kvh, ohn));
    const int ohw = A_HN + (b * 8 + kvh) * 256; int ahw = (wave == 0 && s == 0 && lane < (NUMA ? 2 : 1)) ? ph(ohw, NUMA ? lane : side(0, ohw)) : OOB;
    const int o2w = A_H2 + (b * 8 + kvh) * 2048 + s * 256 + n * 16; int a2w = lane < (NUMA ? 32 : 16) ? ph(o2w, NUMA ? g : side(0, o2w)) : OOB;
    const int col = (kvh * 4 + (s & 3)) * 16;
    int a3[4];
#pragma unroll
    for (int i = 0; i < 4; ++i) { const int o = A_H3 + (b * NFF + i * 14 + (lane >> 2)) * 2048 + col * 4 + (lane & 3) * 16; a3[i] = lane < 56 ? ph(o, side(kvh, o)) : OOB; }
    int aout = n < 4 ? (b * D + col + n * 4 + g) * 2 : OOB;
    const rsrc_t rout = mk_rsrc(out, 4 * D * 2);
#pragma unroll
    for (int i = 0; i < 4; ++i) asm volatile("" : "+v"(a1[i]), "+v"(a2[i]), "+v"(a3[i]));
    asm volatile("" : "+v"(ast), "+v"(ahn), "+v"(ahw), "+v"(a2w), "+v"(aout));
    STAMP(7);
    bar();
    {
      float M, L;
      wg_stats(g, M, L);
      const float f = __builtin_amdgcn_exp2f((*(const float*)(lds + L_ST + g * 64 + wave * 8) - M) * LOG2E);
      *(v4f*)(lds + L_ACC + wave * 2048 + lane * 32) = o0 * f;
      *(v4f*)(lds + L_ACC + wave * 2048 + lane * 32 + 16) = o1 * f;
    }
    FENCE();
    const rsrc_t ro = mk_rsrc(wo + ((size_t)(kvh * 512 + wave * 128) * D + s * 64) * 2, 128 * D * 2 - s * 128);
    const int wooff = (rowb + (n >> 3) * 32) * (D * 2) + (n & 7) * 16;
    v4u wr[2][8];
#pragma unroll
    for (int r = 0; r < 8; ++r) wr[0][r] = LD4(ro, wooff, ((((r >> 1) + 2 * b) & 7) * 4 + (r & 1)) * (D * 2), AUX_NT);
    bar();
#pragma unroll
    for (int r = 0; r < 8; ++r) wr[1][r] = LD4(ro, wooff, (((4 + (r >> 1) + 2 * b) & 7) * 4 + (r & 1)) * (D * 2), AUX_NT);
    FENCE();
    STAMP(8);
    v4u f0[8], f1[8];
    tr8(wr[0], f0);
#pragma unroll
    for (int t = 0; t < 8; ++t) asm volatile("" : "+v"(f0[t]));
    const v2u stt = poll2(rws, ast);
    if (HINT2_EARLY) __builtin_amdgcn_raw_buffer_store_b64(v2u{1u, 1u}, rws, ahw, 0, AUX_SC1);
    STAMP(9);
    pause<DLY1>();
    v4u part[4];
#pragma unroll
    for (int i = 0; i < 4; ++i) part[i] = LD4(rws, a1[i], 0, AUX_SC1);
    FENCE();
    tr8(wr[1], f1);
    const float ms = f_of(stt.x);
    float M = fmaxf(ms, dppf<QP_X1>(ms)); M = fmaxf(M, dppf<QP_X2>(M)); M = fmaxf(M, dppf<ROW_HMIRROR>(M));
    const float fs = __builtin_amdgcn_exp2f((ms - M) * LOG2E);
    float L = fs * f_of(stt.y); L += dppf<QP_X1>(L); L += dppf<QP_X2>(L); L += dppf<ROW_HMIRROR>(L);
    const float wsn = fs * __builtin_amdgcn_rcpf(L);
    const float wsx = dppf<QP_X1>(wsn), wsh = half ? wsx : wsn;
    float wt[4] = {dppf<0x150>(wsh), dppf<0x152>(wsh), dppf<0x154>(wsh), dppf<0x156>(wsh)};
#pragma unroll
    for (int t = 0; t < 8; ++t) asm volatile("" : "+v"(f1[t]));
    asm volatile("" : "+v"(wt[0]), "+v"(wt[1]), "+v"(wt[2]), "+v"(wt[3]));
    FENCE();
    if (__builtin_expect(__builtin_amdgcn_ballot_w64(umax(umax(vmax(part[0]), vmax(part[1])), umax(vmax(part[2]), vmax(part[3]))) == SENT) != 0ull, 0)) gather4(rws, a1, part);
    if (!HINT2_EARLY) __builtin_amdgcn_raw_buffer_store_b64(v2u{1u, 1u}, rws, ahw, 0, AUX_SC1);
    STAMP(10);
    {
      v4f o = __builtin_bit_cast(v4f, part[0]) * wt[0];
#pragma unroll
      for (int i = 1; i < 4; ++i) o += __builtin_bit_cast(v4f, part[i]) * wt[i];
      const u32 op = pk_bf16(t32_add(o.x, o.z), t32_add(o.y, o.w));
      const v4u a0 = {op, dppu<ROW_SHL(1)>(op), dppu<ROW_SHL(2)>(op), dppu<ROW_SHL(3)>(op)};
      const v4u a1v = {dppu<ROW_SHL(4)>(op), dppu<ROW_SHL(5)>(op), dppu<ROW_SHL(6)>(op), dppu<ROW_SHL(7)>(op)};
      v4f dd[8];
#pragma unroll
      for (int t = 0; t < 8; ++t) dd[t] = mfma32(a1v, f1[t], mfma32(a0, f0[t], v4f{0.f, 0.f, 0.f, 0.f}));
      if (lane < 8 || (lane >= 40 && lane < 48)) {
        float* p = (float*)(lds + L_RB + (wave * 2 + half) * 256 + (lane & 7) * 32);
#pragma unroll
        for (int t = 0; t < 8; ++t) p[t] = dd[t][0];
      }
    }
    bar();
    if (wave == 0) {
      const v4f r0 = *(const v4f*)(lds + L_RB + (g * 2) * 256 + n * 16), r1 = *(const v4f*)(lds + L_RB + (g * 2 + 1) * 256 + n * 16);
      v4u o;
#pragma unroll
      for (int r = 0; r < 4; ++r) o[r] = san(x32_add(x16_add(r0[r] + r1[r])));
      ST4(o, rws, a2w, 0);
    }
    STAMP(11);
    if (s < 7) {
      poll2(rws, ahn);
      pause<DLY2>();
      gather4(rws, a2, part);
      STAMP(12);
      const v4f sm = (__builtin_bit_cast(v4f, part[0]) + __builtin_bit_cast(v4f, part[1])) + (__builtin_bit_cast(v4f, part[2]) + __builtin_bit_cast(v4f, part[3]));
      const u32 at = pk_bf16(t32_add(sm.x, sm.z), t32_add(sm.y, sm.w));
      const u32 hp = pk_bf16(bf_lo(xp) + bf_lo(at), bf_hi(xp) + bf_hi(at));
      const v4u a = {hp, dppu<ROW_SHL(1)>(hp), dppu<ROW_SHL(2)>(hp), dppu<ROW_SHL(3)>(hp)};
      v4f dd[8];
#pragma unroll
      for (int t = 0; t < 8; ++t) dd[t] = mfma32(a, fa[t], v4f{0.f, 0.f, 0.f, 0.f});
      *(u32*)(lds + L_H + wave * 256 + (lane & 31) * 8 + half * 4) = hp;
      const float cs0 = bf_lo((u32)__builtin_amdgcn_readfirstlane((int)hp));
      const float d0 = bf_lo(hp) - cs0, d1 = bf_hi(hp) - cs0;
      const float s12 = x16_add(row16_sum(t32_add(d0 + d1, d0 * d0 + d1 * d1)));
      if ((lane & 31) == 0) *(v2f*)(lds + L_LN + wave * 16 + half * 8) = v2f{s12, cs0};
      if (dv) {
        float* p = (float*)(lds + L_RC + (wave * 4 + g) * 128 + (n & 3) * 32);
#pragma unroll
        for (int t = 0; t < 8; ++t) p[t] = dd[t][0];
      }
      bar();
      STAMP(13);
      if (s < 4 && wave == 0) {
        const v2u hq = *(const v2u*)(lds + L_H + (col + (lane & 3) * 4) * 2);
        pause<DLY3>();
        gather4(rws, a3, part);
        STAMP(14);
        v4f r = (__builtin_bit_cast(v4f, part[0]) + __builtin_bit_cast(v4f, part[1])) + (__builtin_bit_cast(v4f, part[2]) + __builtin_bit_cast(v4f, part[3]));
        float y = t16_add(t32_add(r.x, r.z), t32_add(r.y, r.w));
        y += dppf<ROW_ROR4>(y); y += dppf<ROW_ROR8>(y);
        const u32 hw = half ? hq.y : hq.x;
        const float hv = (g & 1) ? bf_hi(hw) : bf_lo(hw);
        const u32 ob = pk_bf16(hv + rbf(y), 0.f);
        __builtin_amdgcn_raw_buffer_store_b16((unsigned short)ob, rout, aout, 0, 0);
      }
    }
  } else {
    const int w4 = wave - 4;
    v4u wb[8], fb[8];
    const rsrc_t r2 = mk_rsrc(w2 + ((size_t)(ug * 32) * D + w4 * 128) * 2, s < 7 ? 32 * D * 2 - w4 * 256 : 0);
    STAMP(6);
    bar();
    STAMP(7);
    float M, L;
    wg_stats(n >> 2, M, L);
    const int ost = A_ST + (grp * 8 + s) * 128 + (n >> 2) * 8;
    const int ast = (wave == 7 && lane < 16 && (lane & 3) == 0) ? ph(ost, side(kvh, ost)) : OOB;
    const int o1w = A_H1 + (grp * 8 + s) * 2048 + w4 * 512 + (lane & 31) * 16, a1t = ph(o1w, side(kvh, o1w));
    int a1w = lane < 32 ? a1t : OOB;
    asm volatile("" : "+v"(a1w));
    const v2u stv = {san(M), san(L)};
    v4f o0, o1;
    pv(__builtin_amdgcn_exp2f((mloc - M) * LOG2E), o0, o1, [&] {
      __builtin_amdgcn_raw_buffer_store_b64(stv, rws, ast, 0, AUX_SC1);
    });
    *(v4f*)(lds + L_ACC + wave * 2048 + lane * 32) = o0;
    *(v4f*)(lds + L_ACC + wave * 2048 + lane * 32 + 16) = o1;
    bar();
    STAMP(8);
    {
      v4f a[4];
#pragma unroll
      for (int i = 0; i < 4; ++i) a[i] = *(const v4f*)(lds + L_ACC + (half * 4 + i) * 2048 + w4 * 512 + (lane & 31) * 16);
      const v4f o = (a[0] + a[1]) + (a[2] + a[3]);
      v4u ou;
#pragma unroll
      for (int r = 0; r < 4; ++r) ou[r] = san(x32_add(o[r]));
      ST4(ou, rws, a1w, 0);
    }
    STAMP(9);
    if (W2_AT == 1) {
#pragma unroll
      for (int r = 0; r < 8; ++r) wb[r] = LD4(r2, (g * 8) * (D * 2) + n * 16, r * (D * 2), AUX_NT);
    }
    bar();
    if (s < 7) {
      if (W2_AT == 2) {
#pragma unroll
        for (int r = 0; r < 8; ++r) wb[r] = LD4(r2, (g * 8) * (D * 2) + n * 16, r * (D * 2), AUX_NT);
      }
      tr8(wb, fb);
      float csum;
      {
        float pp[8];
#pragma unroll
        for (int i = 0; i < 8; ++i) pp[i] = *(const float*)(lds + L_CS + ((lane & 8) + i) * 128 + (g * 8 + (lane & 7)) * 4);
        csum = ((pp[0] + pp[1]) + (pp[2] + pp[3])) + ((pp[4] + pp[5]) + (pp[6] + pp[7]));
        csum += dppf<ROW_ROR8>(csum);
      }
      int a3w[2];
#pragma unroll
      for (int h = 0; h < 2; ++h) { const int o = A_H3 + (b * NFF + ug) * 2048 + w4 * 512 + n * 32 + h * 16; a3w[h] = lane < 16 ? ph(o, side(w4 * 2, o)) : OOB; }
#pragma unroll
      for (int t = 0; t < 8; ++t) asm volatile("" : "+v"(fb[t]));
      asm volatile("" : "+v"(csum), "+v"(a3w[0]), "+v"(a3w[1]));
      STAMP(10);
      bar();
      STAMP(12);
      float pp[8];
#pragma unroll
      for (int i = 0; i < 8; ++i) pp[i] = *(const float*)(lds + L_RC + ((lane & 8) + i) * 128 + (g * 8 + (lane & 7)) * 4);
      const v4f ln = *(const v4f*)(lds + L_LN + (lane & 3) * 16);
      float mean = fmaf(ln.x, 1.f / D, ln.y * 0.25f);
      mean += dppf<QP_X1>(mean); mean += dppf<QP_X2>(mean);
      const float dm = mean - ln.y;
      float var = fmaf(dm, fmaf(128.f, dm, -2.f * ln.x), ln.z);
      var += dppf<QP_X1>(var); var += dppf<QP_X2>(var);
      const float mb = rbf(mean), rs = __builtin_amdgcn_rcpf(rbf(__builtin_amdgcn_sqrtf(rbf(rbf(fmaxf(var, 0.f) * (1.f / D)) + 1e-5f))));
      float ps = ((pp[0] + pp[1]) + (pp[2] + pp[3])) + ((pp[4] + pp[5]) + (pp[6] + pp[7]));
      ps += dppf<ROW_ROR8>(ps);
      const float pre = rbf(fmaf(-mb, csum, ps) * rs);
      const float h1 = pre * __builtin_amdgcn_rcpf(1.f + __builtin_amdgcn_exp2f(-pre * LOG2E));
      const u32 hq = pk_bf16(h1, dppf<ROW_SHL(1)>(h1));
      const v4u a2 = {hq, dppu<ROW_SHL(2)>(hq), dppu<ROW_SHL(4)>(hq), dppu<ROW_SHL(6)>(hq)};
      v4f dd[8];
#pragma unroll
      for (int t = 0; t < 8; ++t) dd[t] = mfma32(a2, fb[t], v4f{0.f, 0.f, 0.f, 0.f});
      ST4((v4u{san(dd[0][0]), san(dd[1][0]), san(dd[2][0]), san(dd[3][0])}), rws, a3w[0], 0);
      ST4((v4u{san(dd[4][0]), san(dd[5][0]), san(dd[6][0]), san(dd[7][0])}), rws, a3w[1], 0);
      STAMP(13);
    }
  }
}

__device__ __forceinline__ u64 rt_() { u64 t; asm volatile("s_memrealtime %0\n\ts_waitcnt lgkmcnt(0)" : "=s"(t) :: "memory"); return t; }
template <int WHAT, int AUX, int Q>
__global__ void __launch_bounds__(512, 2) k_x(const char* kc, const char* vc, u32* sink) {
  u64 t0; asm volatile("s_memrealtime %0" : "=s"(t0) :: "memory");
  asm volatile("s_waitcnt lgkmcnt(0)" :: "s"(kc), "s"(vc), "s"(sink) : "memory");
  const u64 ta = rt_();
  const int wg = blockIdx.x, t = threadIdx.x, wave = __builtin_amdgcn_readfirstlane(t >> 6), lane = t & 63;
  const int kvh = wg & 7, s = (wg >> 3) & 7, b = wg >> 6;
  const size_t base = ((size_t)(b * 8 + kvh) * SK + (SK - WIN) + s * 512) * DH * 2;
  const rsrc_t rk = mk_rsrc(kc + base, 512 * 256), rv = mk_rsrc(vc + base, 512 * 256);
  v4u r[32]; const int c0 = wave * 16 + (wg >> 3) * 4;
  u64 tb = 0;
#pragma unroll
  for (int i = 0; i < 32; ++i) {
    r[i] = v4u{0, 0, 0, 0};
    if (i < 16 && (WHAT & 1)) r[i] = LD4(rk, lane * 16, ((c0 + i) & 127) * 1024, AUX);
    if (i >= 16 && (WHAT & 2)) r[i] = LD4(rv, lane * 16, ((c0 + i - 16) & 127) * 1024, AUX);
    if (Q == 2 && i == 0) { FENCE(); asm volatile("s_waitcnt vmcnt(0)" ::: "memory"); tb = rt_(); FENCE(); }
  }
  FENCE();
  v4u a = {0, 0, 0, 0};
#pragma unroll
  for (int i = 0; i < 32; ++i) a ^= r[i];
  u32 x = a.x ^ a.y ^ a.z ^ a.w; asm volatile("" : "+v"(x)); FENCE();
  const u64 t1 = rt_();
  if (x == 0x12345678u) sink[0] = x;
  const u64 until = t1 + ((Q == 0 ? t1 : Q == 1 ? ta : tb) - t0) * 50ull;
  for (int w_ = 0; w_ < 400000; ++w_) { if (rt_() >= until) break; __builtin_amdgcn_s_sleep(4); }
}

constexpr int g_pol = 0;
extern "C" void kernel_launch(void* const* d_in, const int* in_sizes, int n_in, void* d_out, int out_size, void* d_ws, size_t ws_size,
                              hipStream_t stream) {
  k_x<3,2,1><<<256,512,0,stream>>>((const char*)d_in[1],(const char*)d_in[2],(u32*)((char*)d_ws+(16<<20)));
  fused_decode_kernel<<<NWG, 512, 0, stream>>>((const u32*)d_in[0], (const char*)d_in[1], (const char*)d_in[2], (const char*)d_in[3],
                                               (const char*)d_in[4], (const char*)d_in[6], (const char*)d_in[7], (u32*)d_out, (char*)d_ws, (u64)g_pol);
}
```

```cpp
#define CODE_BYTES 19200
#define T_H2 124u
#define T_H3 176u
#define T_H4 268u
#include <hip/hip_runtime.h>
#include <hip/hip_bf16.h>
#include <stdint.h>
#include <type_traits>

typedef unsigned u32;
typedef unsigned long long u64;
typedef u32 v8u __attribute__((ext_vector_type(8)));
typedef u32 v4u __attribute__((ext_vector_type(4)));
typedef u32 v2u __attribute__((ext_vector_type(2)));
typedef float v4f __attribute__((ext_vector_type(4)));
typedef float v2f __attribute__((ext_vector_type(2)));
typedef __bf16 bf16x8 __attribute__((ext_vector_type(8)));
typedef __bf16 bf16x2 __attribute__((ext_vector_type(2)));
typedef short s16x4 __attribute__((ext_vector_type(4)));
typedef __amdgpu_buffer_rsrc_t rsrc_t;
typedef volatile __attribute__((address_space(3))) u32 lds_vu32;

constexpr int D = 512, HKV = 8, SK = 16384, DH = 128, WIN = 4096, FF = 1792, QD = 4096;
constexpr int NWG = 256;
constexpr int AUX_NT = 2, AUX_SC1 = 16;
constexpr int OOB = 0x7FFF0000;
constexpr u32 SPIN_CAP = 1u << 20;
constexpr u32 KEEP = 0xFFFFFF00u;
constexpr float LOG2E = 1.4426950408889634f;

#define PACE_T 56u
#define PACE_O 20u
constexpr u32 T_HINT_R = 70u, T_HINT_F = 110u, T_W2 = 20u, T_ORIGIN = 240u;
constexpr int G_SLEEP = 9, W1_SLEEP = 10;
constexpr int W_START = 6, W_MAX = 11, QK_AT = 16, PM_AT = 25;
constexpr int qk_done(int i) { const int a = 2 * (i - QK_AT + 1), c = i - W_MAX + 1, m = a < c ? a : c; return m < 0 ? 0 : m > 16 ? 16 : m; }

constexpr int WS_E = 0;
constexpr int WS_VT = 4096;
constexpr int WS_OR = 8192;
constexpr int WS_HN = 16384;
constexpr int WS_H3 = WS_HN + 16384;
constexpr int WS_ST = WS_H3 + 16384;
constexpr int WS_PR = 98304;
constexpr int WS_QP = 163840;
constexpr int WS_H1 = WS_QP + 32 * 4 * 2048;
constexpr int WS_H2 = WS_H1 + NWG * 2048;
constexpr int WS_H4 = WS_H2 + 128 * 4096;
constexpr int WS_END = WS_H4 + 128 * 4096;
static_assert(WS_ST + NWG * 128 <= WS_PR && WS_PR + 65536 <= WS_QP && WS_END <= (2 << 20), "layout");
__device__ __forceinline__ int sd(int o, int side) { return ((o >> 13) << 14) | ((side & 1) << 13) | (o & 8191); }
__device__ __forceinline__ int partner(int c, int pm) { return ((pm ^ (pm >> 1)) & 5) ? (((c + 7) ^ 1) + 1) & 7 : c ^ 1; }

constexpr int L_Q = 0;
constexpr int L_FLAG = 1024;
constexpr int L_ST = 1088;
constexpr int L_FW = 1344;
constexpr int L_O = 1536;
constexpr int L_ACC = 2048;
constexpr int L_RC = L_ACC + 16384;
constexpr int L_CS = L_RC + 2048;
constexpr int L_LN = L_CS + 512;
constexpr int L_DUM = L_LN + 128;
constexpr int L_END = L_DUM + 512;

#define FENCE() __builtin_amdgcn_sched_barrier(0)
#define CLOB() asm volatile("" ::: "memory")
#define STAMP(i) do {} while (0)
#define RETRY(i, c) do {} while (0)
#define LD4(r, v, s, aux) __builtin_amdgcn_raw_buffer_load_b128(r, v, s, aux)
#define LD2(r, v, s) __builtin_amdgcn_raw_buffer_load_b64(r, v, s, AUX_SC1)
#define ST4(d, r, v, s) __builtin_amdgcn_raw_buffer_store_b128(d, r, v, s, AUX_SC1)
#define ST2(d, r, v, s) __builtin_amdgcn_raw_buffer_store_b64(d, r, v, s, AUX_SC1)
#define ZERO4 (v4f{0.f, 0.f, 0.f, 0.f})
#define VTMP "v200","v201","v202","v203","v204","v205","v206","v207","v208","v209","v210","v211","v212","v213","v214","v215","v216","v217","v218","v219", \
  "v220","v221","v222","v223","v224","v225","v226","v227","v228","v229","v230","v231","v232","v233","v234","v235","v236","v237","v238","v239","v240","v241", \
  "v242","v243","v244","v245","v246","v247","v248","v249","v250","v251","v252","v253","v254","v255"
#define DPPM " row_mask:0xf bank_mask:0xf bound_ctrl:1\n\t"
#define MF(d, f) "v_mfma_f32_16x16x32_bf16 v[" d "], v[232:235], %[" f "], 0\n\t"
#define FRAGS(f) [f0] "v"(f[0]), [f1] "v"(f[1]), [f2] "v"(f[2]), [f3] "v"(f[3]), [f4] "v"(f[4]), [f5] "v"(f[5]), [f6] "v"(f[6]), [f7] "v"(f[7])
#define TAGS [t0] "v"(T0), [t1] "v"(T1), [t2] "v"(T2), [t3] "v"(T3), [m] "s"(KEEP)

__device__ __forceinline__ rsrc_t mk_rsrc(const void* p, u32 bytes) { return __builtin_amdgcn_make_buffer_rsrc((void*)p, 0, bytes, 0x00020000); }
__device__ __forceinline__ char* ptr_of(u32 lo, u32 hi) { return (char*)(__attribute__((address_space(1))) char*)(((u64)hi << 32) | lo); }
__device__ __forceinline__ u32 uni(u32 x) { return (u32)__builtin_amdgcn_readfirstlane((int)x); }
__device__ __forceinline__ u64 ballot(bool c) { return __builtin_amdgcn_ballot_w64(c); }
__device__ __forceinline__ float f_of(u32 x) { return __builtin_bit_cast(float, x); }
__device__ __forceinline__ u32 u_of(float x) { return __builtin_bit_cast(u32, x); }
__device__ __forceinline__ v4f f4(v4u x) { return __builtin_bit_cast(v4f, x); }
__device__ __forceinline__ u32 pk_bf16(float lo, float hi) { v2f t = {lo, hi}; return __builtin_bit_cast(u32, __builtin_convertvector(t, bf16x2)); }
__device__ __forceinline__ float bf_lo(u32 x) { return f_of(x << 16); }
__device__ __forceinline__ float bf_hi(u32 x) { return f_of(x & 0xFFFF0000u); }
__device__ __forceinline__ float rbf(float x) { return bf_lo(pk_bf16(x, x)); }
__device__ __forceinline__ u32 or4(v4u v) { return (v.x | v.y) | (v.z | v.w); }
template <int CTRL> __device__ __forceinline__ u32 dppu(u32 x) { return (u32)__builtin_amdgcn_mov_dpp((int)x, CTRL, 0xf, 0xf, true); }
template <int CTRL> __device__ __forceinline__ float dppf(float x) { return f_of(dppu<CTRL>(u_of(x))); }
constexpr int QP_X1 = 0xB1, QP_X2 = 0x4E, ROW_ROR4 = 0x124, ROW_ROR8 = 0x128, ROW_HMIRROR = 0x141;
__device__ __forceinline__ float t32_add(float a, float b) { auto s = __builtin_amdgcn_permlane32_swap(u_of(a), u_of(b), false, false); return f_of(s[0]) + f_of(s[1]); }
__device__ __forceinline__ float t16_add(float a, float b) { auto s = __builtin_amdgcn_permlane16_swap(u_of(a), u_of(b), false, false); return f_of(s[0]) + f_of(s[1]); }
__device__ __forceinline__ float x16_max(float x) { auto s = __builtin_amdgcn_permlane16_swap(u_of(x), u_of(x), false, false); return fmaxf(f_of(s[0]), f_of(s[1])); }
__device__ __forceinline__ float x32_max(float x) { auto s = __builtin_amdgcn_permlane32_swap(u_of(x), u_of(x), false, false); return fmaxf(f_of(s[0]), f_of(s[1])); }
__device__ __forceinline__ float quad_rows_sum(float x) { x += dppf<QP_X1>(x); x += dppf<QP_X2>(x); x = t16_add(x, x); return t32_add(x, x); }
__device__ __forceinline__ v4f mfma32(v4u a, v4u b, v4f c) { return __builtin_amdgcn_mfma_f32_16x16x32_bf16(__builtin_bit_cast(bf16x8, a), __builtin_bit_cast(bf16x8, b), c, 0, 0, 0); }
__device__ __forceinline__ v4f mfma16(v2u a, v2u b, v4f c) { return __builtin_amdgcn_mfma_f32_16x16x16bf16_1k(__builtin_bit_cast(s16x4, a), __builtin_bit_cast(s16x4, b), c, 0, 0, 0); }
__device__ __forceinline__ void bar() { FENCE(); asm volatile("s_waitcnt lgkmcnt(0)\n\ts_barrier" ::: "memory"); FENCE(); }
template <int N, typename F> __device__ __forceinline__ void unroll(F&& f) {
  if constexpr (N > 0) { unroll<N - 1>(f); f(std::integral_constant<int, N - 1>{}); }
}
template <int N> __device__ __forceinline__ void pin(v4u (&f)[N]) {
#pragma unroll
  for (int t = 0; t < N; ++t) asm volatile("" : "+v"(f[t]));
}

__device__ __forceinline__ void tr8(const v4u (&w)[8], v4u (&f)[8]) {
#pragma unroll
  for (int t = 0; t < 8; ++t)
#pragma unroll
    for (int i = 0; i < 4; ++i) f[t][i] = __builtin_amdgcn_perm(w[2 * i + 1][t >> 1], w[2 * i][t >> 1], (t & 1) ? 0x07060302u : 0x05040100u);
}
__device__ __forceinline__ v2u next_epoch(v2u old) {
  const u64 e = ((u64)uni(old.y) << 32) | uni(old.x);
  u64 z = e * 0x9E3779B97F4A7C15ull + 0xD1B54A32D192ED03ull;
  z ^= z >> 29; z *= 0xBF58476D1CE4E5B9ull; z ^= z >> 32;
  z = e ^ (z | 0x0101010101010101ull);
  return v2u{(u32)z, (u32)(z >> 32)};
}
__device__ __forceinline__ u32 lds_wait(const char* p) {
  u32 v = 0;
#pragma unroll 1
  for (u32 spins = 0; spins < SPIN_CAP * 8u; ++spins) {
    v = *(lds_vu32*)p;
    if (ballot(v == 0u) == 0ull) break;
    __builtin_amdgcn_s_sleep(1);
  }
  CLOB();
  return uni(v);
}
__device__ __forceinline__ u32 now() { u64 t; asm volatile("s_memrealtime %0\n\ts_waitcnt lgkmcnt(0)" : "=s"(t) :: "memory"); return (u32)t; }
__device__ __forceinline__ void wait_until(u32 t) {
  u32 cnt = 256;
  asm volatile("1:\n\ts_memrealtime s[90:91]\n\ts_sub_u32 %[c], %[c], 1\n\ts_cbranch_scc1 2f\n\ts_waitcnt lgkmcnt(0)\n\t"
               "s_sub_u32 s90, s90, %[t]\n\ts_cmp_lt_i32 s90, 0\n\ts_cbranch_scc1 1b\n\t2:\n\ts_waitcnt lgkmcnt(0)"
               : [c] "+s"(cnt) : [t] "s"(t) : "memory", "scc", "s90", "s91");
}
__device__ __forceinline__ u32 poll_times(const char* base, int a, u32 tag, u32 lanes, u32 ref) {
  u32 cnt = SPIN_CAP, d0, d1;
  asm volatile(
      "s_mov_b32 exec_lo, %[ln]\n\ts_mov_b32 exec_hi, 0\n\t"
      "1:\n\tglobal_load_dwordx2 v[200:201], %[a], %[b] sc1\n\ts_sub_u32 %[c], %[c], 1\n\ts_cbranch_scc1 2f\n\t"
      "s_waitcnt vmcnt(0)\n\tv_cmpx_ne_u32 vcc, %[tag], v201\n\ts_cbranch_execnz 1b\n\t"
      "2:\n\ts_waitcnt vmcnt(0)\n\ts_mov_b64 exec, -1\n\t"
      "v_subrev_u32 v202, %[ref], v200\n\tv_bfe_i32 v202, v202, 0, 16\n\ts_nop 1\n\t"
      "v_max_i32_dpp v202, v202, v202 quad_perm:[1,0,3,2]" DPPM "s_nop 1\n\t"
      "v_max_i32_dpp v202, v202, v202 quad_perm:[2,3,0,1]" DPPM "s_nop 1\n\t"
      "v_max_i32_dpp v202, v202, v202 row_half_mirror" DPPM "s_cmp_eq_u32 %[ln], 0xff\n\t"
      "v_readlane_b32 %[d0], v202, 0\n\ts_cbranch_scc1 3f\n\t"
      "v_readlane_b32 %[d1], v202, 8\n\ts_max_i32 %[d0], %[d0], %[d1]\n\t"
      "v_readlane_b32 %[d1], v202, 16\n\ts_max_i32 %[d0], %[d0], %[d1]\n\t"
      "v_readlane_b32 %[d1], v202, 24\n\ts_max_i32 %[d0], %[d0], %[d1]\n\t"
      "3:\n\ts_add_u32 %[d0], %[d0], %[ref]"
      : [c] "+s"(cnt), [d0] "=&s"(d0), [d1] "=&s"(d1)
      : [a] "v"(a), [b] "s"(base), [tag] "s"(tag), [ln] "s"(lanes), [ref] "s"(ref)
      : "memory", "vcc", "scc", "v200", "v201", "v202");
  return d0;
}
template <int S> __device__ __forceinline__ u32 reduce4(const char* base, int a, u32 T0, u32 T1, u32 T2, u32 T3, u32 exhi, float add, u32& left) {
  u32 y, cnt = SPIN_CAP;
  asm volatile(
      "1:\n\ts_mov_b32 exec_hi, %[ex]\n\t"
      "global_load_dwordx4 v[200:203], %[a], %[b] sc1\n\tglobal_load_dwordx4 v[204:207], %[a], %[b] offset:%[s1] sc1\n\t"
      "global_load_dwordx4 v[208:211], %[a], %[b] offset:%[s2] sc1\n\tglobal_load_dwordx4 v[212:215], %[a], %[b] offset:%[s3] sc1\n\t"
      "s_mov_b32 exec_hi, -1\n\tv_mov_b32 v216, 0\n\tv_mov_b32 v217, 0\n\tv_mov_b32 v218, 0\n\tv_mov_b32 v219, 0\n\ts_mov_b32 exec_hi, %[ex]\n\t"
      "s_sub_u32 %[c], %[c], 1\n\t"
      "s_waitcnt vmcnt(3)\n\t"
      "v_xor_b32 v200, v200, %[t0]\n\tv_xor_b32 v201, v201, %[t1]\n\tv_xor_b32 v202, v202, %[t2]\n\tv_xor_b32 v203, v203, %[t3]\n\t"
      "s_waitcnt vmcnt(2)\n\t"
      "v_xor_b32 v204, v204, %[t0]\n\tv_xor_b32 v205, v205, %[t1]\n\tv_xor_b32 v206, v206, %[t2]\n\tv_xor_b32 v207, v207, %[t3]\n\t"
      "v_add_f32 v216, v200, v204\n\tv_add_f32 v217, v201, v205\n\tv_add_f32 v218, v202, v206\n\tv_add_f32 v219, v203, v207\n\t"
      "s_waitcnt vmcnt(1)\n\t"
      "v_xor_b32 v208, v208, %[t0]\n\tv_xor_b32 v209, v209, %[t1]\n\tv_xor_b32 v210, v210, %[t2]\n\tv_xor_b32 v211, v211, %[t3]\n\t"
      "v_add_f32 v216, v216, v208\n\tv_add_f32 v217, v217, v209\n\tv_add_f32 v218, v218, v210\n\tv_add_f32 v219, v219, v211\n\t"
      "s_waitcnt vmcnt(0)\n\t"
      "v_xor_b32 v212, v212, %[t0]\n\tv_xor_b32 v214, v214, %[t2]\n\tv_xor_b32 v213, v213, %[t1]\n\tv_xor_b32 v215, v215, %[t3]\n\t"
      "v_add_f32 v216, v216, v212\n\tv_add_f32 v218, v218, v214\n\tv_add_f32 v217, v217, v213\n\tv_add_f32 v219, v219, v215\n\t"
      "s_mov_b32 exec_hi, -1\n\t"
      "v_permlane32_swap_b32 v216, v218\n\tv_permlane32_swap_b32 v217, v219\n\t"
      "v_add_f32 v216, v216, v218\n\tv_add_f32 v217, v217, v219\n\t"
      "v_or3_b32 v220, v200, v201, v202\n\tv_or3_b32 v221, v203, v204, v205\n\t"
      "v_permlane16_swap_b32 v216, v217\n\tv_add_f32 v216, v216, v217\n\t"
      "v_or3_b32 v220, v220, v206, v207\n\tv_or3_b32 v221, v221, v208, v209\n\t"
      "v_add_f32_dpp v216, v216, v216 row_ror:4" DPPM
      "v_or3_b32 v220, v220, v210, v211\n\tv_or3_b32 v221, v221, v212, v213\n\t"
      "v_add_f32_dpp v216, v216, v216 row_ror:8" DPPM
      "v_or3_b32 v220, v220, v214, v215\n\ts_mov_b32 exec_hi, %[ex]\n\tv_bitop3_b32 v220, v220, v221, %[m] bitop3:0x54\n\tv_cmp_ne_u32 vcc, 0, v220\n\t"
      "s_mov_b32 exec_hi, -1\n\t"
      "v_cvt_pk_bf16_f32 v216, v216, v216\n\tv_lshlrev_b32 v216, 16, v216\n\tv_add_f32 v216, %[add], v216\n\tv_cvt_pk_bf16_f32 %[y], v216, v216\n\t"
      "s_cbranch_scc1 2f\n\ts_cbranch_vccnz 1b\n\t2:"
      : [y] "=&v"(y), [c] "+s"(cnt)
      : [a] "v"(a), [b] "s"(base), TAGS, [ex] "s"(exhi), [add] "v"(add), [s1] "i"(S), [s2] "i"(2 * S), [s3] "i"(3 * S)
      : "memory", "vcc", "scc", VTMP);
  left = cnt;
  return y;
}

__device__ __forceinline__ u64 pace_table() {
  u64 base;
#define PE(k) "s_waitcnt vmcnt(" #k ")\n\ts_setpc_b64 s[94:95]\n\t"
  asm volatile("s_getpc_b64 %0\n\ts_branch 1f\n\t" PE(3) PE(4) PE(5) PE(6) PE(7) PE(8) PE(9) PE(10) "1:" : "=s"(base));
  return base + 4;
}
template <int I, bool LATE> __device__ __forceinline__ void pace_wait(u64 target) {
  if (I >= W_START && I < 8 && !LATE) asm volatile("s_waitcnt vmcnt(%0)" :: "i"(W_START - 1) : "memory");
  else if (I >= W_START) asm volatile("s_swappc_b64 s[94:95], %0" :: "s"(target) : "memory", "s94", "s95");
}
template <int I> __device__ __forceinline__ void pace_check(u32& due, u32& lvl, u64& target, u64 table, u32 step) {
  if (I >= 7 && I < 31 && (I & 3) == 3) {
    asm volatile("s_memrealtime s[90:91]\n\ts_mul_i32 %0, %0, 14\n\ts_sub_u32 %0, %0, %1\n\ts_waitcnt lgkmcnt(0)\n\ts_add_u32 %0, %0, s90\n\t"
                 "s_ashr_i32 %0, %0, 4\n\ts_max_i32 %0, %0, 1\n\ts_min_i32 %0, %0, 7"
                 : "+s"(lvl) : "s"(due) : "memory", "scc", "s90", "s91");
    target = table + lvl * 8u;
    due += step;
  }
}

template <int I> __device__ __forceinline__ float quad_sum(v4f d) {
  if (I == 0) return (d[0] + dppf<0x55>(d[1])) + dppf<0xAA>(d[2]) + dppf<0xFF>(d[3]);
  if (I == 1) return (d[1] + dppf<0x00>(d[0])) + dppf<0xAA>(d[2]) + dppf<0xFF>(d[3]);
  if (I == 2) return (d[2] + dppf<0x00>(d[0])) + dppf<0x55>(d[1]) + dppf<0xFF>(d[3]);
  return (d[3] + dppf<0x00>(d[0])) + dppf<0x55>(d[1]) + dppf<0xAA>(d[2]);
}
__device__ __forceinline__ float score4(float t0, float t1, float t2, float t3, int lane) {
  const float a = (lane & 1) ? t1 : t0, c = (lane & 1) ? t3 : t2;
  return rbf(rbf((lane & 2) ? c : a) * 0.08838834764831845f);
}
__device__ __forceinline__ void softmax64(const float (&S)[4], char* lds, int wave, int lane, u32 (&P)[4]) {
  float m = fmaxf(fmaxf(S[0], S[1]), fmaxf(S[2], S[3]));
  m = fmaxf(m, dppf<QP_X1>(m)); m = fmaxf(m, dppf<QP_X2>(m)); m = x32_max(x16_max(m));
  float l = 0.f;
#pragma unroll
  for (int k = 0; k < 4; ++k) { const float e = __builtin_amdgcn_exp2f((S[k] - m) * LOG2E); l += e; P[k] = pk_bf16(e, e); }
  l = quad_rows_sum(l);
  *(float*)(lds + L_ST + (lane >> 5) * 128 + ((lane & 15) >> 2) * 32 + wave * 4) = (lane >> 5) ? l : m;
}
__device__ __forceinline__ void pv_store(const u32 (&P)[4], u32 m0, u32 m1, const v4u (&V)[16], char* dst) {
  v4f o0 = ZERO4, o1 = ZERO4;
  v2u A[16];
  auto make = [&](auto ic) { constexpr int i = decltype(ic)::value; const u32 pb = dppu<0x55 * (i & 3)>(P[i >> 2]); A[i] = v2u{pb & m0, pb & m1}; asm volatile("" : "+v"(A[i])); };
  make(std::integral_constant<int, 0>{});
  unroll<16>([&](auto ic) {
    constexpr int i = decltype(ic)::value;
    if constexpr (i < 15) make(std::integral_constant<int, i + 1>{});
    FENCE();
    o0 = mfma16(A[i], v2u{V[i].x, V[i].y}, o0); o1 = mfma16(A[i], v2u{V[i].z, V[i].w}, o1);
    FENCE();
  });
  *(v4f*)dst = o0;
  *(v4f*)(dst + 16) = o1;
}

__global__ void __launch_bounds__(512, 2)
fused_decode_kernel(const char*, const char*, char* ws_arg, const char*, const char*, const char*, const char*, const char*, char*
) {
  __shared__ __attribute__((aligned(16))) char lds[L_END];
  STAMP(0);
  u32 warm;
  asm volatile("s_load_dword %0, %1, 0x0" : "={s96}"(warm) : "s"(__builtin_amdgcn_kernarg_segment_ptr()) : "memory");
  FENCE();
  const int wg = blockIdx.x, tid = threadIdx.x, lane = tid & 63;
  const int wave = __builtin_amdgcn_readfirstlane(tid >> 6);
  u32 code_pf = 0;
  if (wave == 4) {
    u64 pc;
    asm volatile("s_getpc_b64 %0" : "=s"(pc));
    code_pf = __builtin_amdgcn_raw_buffer_load_b32(mk_rsrc((const char*)(pc & ~127ull), CODE_BYTES), lane * 512, ((wg >> 3) & 3) * 128, 0);
    *(lds_vu32*)(lds + L_FLAG + (lane & 15) * 4) = 0u;
  }
  FENCE();
  const int col = wg & 7, j = wg >> 3, s = j & 7, b = j >> 3;
  const int g = lane >> 4, n = lane & 15, half = lane >> 5;
  const int grp = col * 4 + b, role = j & 3, rbk = j >> 2, ug = col * 7 + rbk;
  const bool roleB = (j & 1) == 0, roleF = role == 3 && j < 28;
  const int LDS0 = (int)(size_t)(__attribute__((address_space(3))) char*)lds;
  const u64 table = pace_table();
  u64 target = 0;
  u32 due = 2u * PACE_T + PACE_O, lvl = W_START - 4;
  v4u K[16], V[16];
#define STREAM_PREP() \
  int koff = ((lane >> 2) & 3) * 256 + g * 64 + (lane & 3) * 16, voff = lane * 16; \
  int so[4], su[4]; \
  _Pragma("unroll") for (int q = 0; q < 4; ++q) { so[q] = ((q * 8 + wave + j) & 31) * 4096; su[q] = ((q + wave) & 3) * 1024; } \
  u32 kvbase = (u32)((b * HKV + col) * SK + (SK - WIN) + s * 512) * (DH * 2); \
  asm volatile("" : "+v"(koff), "+v"(voff)); \
  asm volatile("" : "+s"(so[0]), "+s"(so[1]), "+s"(so[2]), "+s"(so[3]), "+s"(su[0]), "+s"(su[1]), "+s"(su[2]), "+s"(su[3]), "+s"(kvbase))
#define ARGS() \
  v8u ka; v4u kb4; v2u kc2; \
  asm volatile("s_load_dwordx8 %0, %3, 0x0\n\ts_load_dwordx4 %1, %3, 0x20\n\ts_load_dwordx2 %2, %3, 0x30\n\ts_waitcnt lgkmcnt(0)" \
               : "=&s"(ka), "=&s"(kb4), "=&s"(kc2) : "s"(__builtin_amdgcn_kernarg_segment_ptr()), "{s96}"(warm) : "memory"); \
  const char* const kc = ptr_of(ka[0], ka[1]); const char* const vc = ptr_of(ka[2], ka[3]); char* const ws = (char*)(((u64)ptr_of(ka[4], ka[5]) + 0x1FFFFFull) & ~0x1FFFFFull); \
  const char* const x = ptr_of(ka[6], ka[7]); const char* const wq = ptr_of(kb4[0], kb4[1]); const char* const wo = ptr_of(kb4[2], kb4[3]); \
  const char* const w1 = ptr_of(kc2[0], kc2[1]); \
  const rsrc_t rws = mk_rsrc(ws, WS_END)
#define KV_RSRC() const rsrc_t rk = mk_rsrc(kc + kvbase, 512 * 256), rv = mk_rsrc(vc + kvbase, 512 * 256)
#define STREAM(i, late) do { pace_wait<i, late>(target); \
    if ((i) < 16) K[(i) & 15] = LD4(rk, koff, so[((i) & 15) >> 2] + su[(i) & 3], AUX_NT); \
    else V[(i) & 15] = LD4(rv, voff, so[((i) & 15) >> 2] + su[(i) & 3], AUX_NT); \
    FENCE(); pace_check<i>(due, lvl, target, table, PACE_T); } while (0)
#define MAKE_TAGS() \
  u32 T0 = ep.x & 255u, T1 = (ep.x >> 8) & 255u, T2 = (ep.x >> 16) & 255u, T3 = ep.x >> 24; \
  asm volatile("" : "+v"(T0), "+v"(T1), "+v"(T2), "+v"(T3))
  const int lq = L_Q + (n >> 2) * 256 + g * 64 + (n & 3) * 16;
  const int e_off = lane == 0 ? 0 : OOB;
  u32 pm0 = (lane & 3) == 0 ? 0x0000FFFFu : (lane & 3) == 1 ? 0xFFFF0000u : 0u, pm1 = (lane & 3) == 2 ? 0x0000FFFFu : (lane & 3) == 3 ? 0xFFFF0000u : 0u;

  if (__builtin_expect(wave < 4, 1)) {
    STREAM_PREP();
    FENCE();
    ARGS();
    KV_RSRC();
    int dcur = 0;
    const int aq = WS_QP + grp * 8192 + half * 2048 + wave * 512 + (lane & 31) * 16;
    unroll<32>([&](auto ic) {
      constexpr int i = decltype(ic)::value;
      if (i == W_START) { STAMP(1); due += now(); asm volatile("s_barrier" ::: "memory"); }
      STREAM(i, false);
      if (i >= 18 && i < 27 && (i & 3) == 2) { const int dt = (int)uni(*(lds_vu32*)(lds + L_FLAG + 36)); due += dt - dcur; dcur = dt; }
    });
    const v2u eold = LD2(rws, e_off, WS_E);
    v4u qp[2] = {LD4(rws, aq, 0, AUX_SC1), LD4(rws, aq, 4096, AUX_SC1)};
    FENCE();
    *(lds_vu32*)(lds + L_FLAG + wave * 4) = lvl;
    FENCE();
    const v2u ep = next_epoch(eold);
    {
      const u32 e0 = (n & 1) ? ep.y : ep.x;
      const v4u Tq = {e0 & 15u, (e0 >> 8) & 15u, (e0 >> 16) & 15u, (e0 >> 24) & 15u};
      qp[0] ^= Tq; qp[1] ^= Tq;
      if (__builtin_expect(ballot(((or4(qp[0]) | or4(qp[1])) & 15u) != 0u) != 0ull, 0)) {
#pragma unroll 1
        for (u32 spins = 0; spins < SPIN_CAP; ++spins) {
          CLOB();
          qp[0] = LD4(rws, aq, 0, AUX_SC1) ^ Tq; qp[1] = LD4(rws, aq, 4096, AUX_SC1) ^ Tq;
          if (ballot(((or4(qp[0]) | or4(qp[1])) & 15u) != 0u) == 0ull) break;
        }
        RETRY(13, true);
      }
      const v4f q4 = f4(qp[0]) + f4(qp[1]);
      *(u32*)(lds + L_Q + wave * 256 + (g & 1) * 128 + (n & 7) * 16 + (n >> 3) * 8 + half * 4) = pk_bf16(t32_add(q4.x, q4.z), t32_add(q4.y, q4.w));
      asm volatile("s_waitcnt lgkmcnt(0)" ::: "memory");
      *(lds_vu32*)(lds + L_FLAG + 16 + wave * 4) = 1u;
    }
    lds_wait(lds + L_FLAG + 16 + (lane & 3) * 4);
    {
      const v4u qb = *(const v4u*)(lds + lq);
      v4f d[16];
#pragma unroll
      for (int i = 0; i < 16; ++i) d[i] = mfma32(K[i], qb, ZERO4);
      FENCE();
      float t[16];
      unroll<16>([&](auto ic) { constexpr int i = decltype(ic)::value; t[i] = quad_sum<i & 3>(d[i]); });
      const float S[4] = {score4(t[0], t[1], t[2], t[3], lane), score4(t[4], t[5], t[6], t[7], lane), score4(t[8], t[9], t[10], t[11], lane), score4(t[12], t[13], t[14], t[15], lane)};
      u32 P[4];
      softmax64(S, lds, wave, lane, P);
      pv_store(P, pm0, pm1, V, lds + L_ACC + wave * 2048 + lane * 32);
    }
    if (wave == 0) {
      v4u vt;
#pragma unroll 1
      for (u32 spins = 0; spins < SPIN_CAP; ++spins) {
        CLOB();
        vt = LD4(rws, lane * 16, WS_VT, AUX_SC1);
        if (ballot(((((vt.x ^ ep.x) | (vt.z ^ ep.x)) >> 2) | (vt.y ^ ep.y) | (vt.w ^ ep.y)) != 0u) == 0ull) break;
        RETRY(2, true);
      }
      const int ones = __builtin_popcountll(ballot((vt.x & 1u) != 0u)) + __builtin_popcountll(ballot((vt.z & 1u) != 0u));
      const u32 pv = ((u32)ballot((vt.x & 2u) != 0u) & 15u) ^ (ones > 64 ? 15u : 0u);
      *(lds_vu32*)(lds + L_FLAG + 32) = 256u | pv | ((pv ^ 15u) << 4);
    }
    const int pm = (int)lds_wait(lds + L_FLAG + 32) & 255, side = pm >> col, kb = partner(col, pm);
    v4u late_args;
    asm volatile("s_load_dwordx4 %0, %1, 0x38\n\ts_waitcnt lgkmcnt(0)" : "=&s"(late_args) : "s"(__builtin_amdgcn_kernarg_segment_ptr()) : "memory");
    const char* const w2 = ptr_of(late_args.x, late_args.y);
    char* const out = ptr_of(late_args.z, late_args.w);
    MAKE_TAGS();
    int ast = lane < 8 ? WS_ST + sd(((grp & 15) * 8 + s) * 128, side) + lane * 16 : OOB;
    int lst = L_ST + (lane & 3) * 32 + ((lane >> 2) & 7) * 4;
    int ahn = WS_HN + sd(((col & 3) * 8 + (lane & 7)) * 256, side);
    asm volatile("" : "+v"(ast), "+v"(lst), "+v"(ahn));
    u32 tb2;
    auto to_b2 = [&] {
      FENCE();
      v4u stv = {0u, 0u, 0u, 0u};
      if (wave == 3) {
        const float mw = *(const float*)(lds + lst), lw = *(const float*)(lds + lst + 128);
        float M = fmaxf(mw, dppf<ROW_ROR4>(mw)); M = fmaxf(M, dppf<ROW_ROR8>(M)); M = x16_max(M);
        const float fw = __builtin_amdgcn_exp2f((mw - M) * LOG2E);
        *(float*)(lds + lst + (L_FW - L_ST)) = fw;
        float L = fw * lw; L += dppf<ROW_ROR4>(L); L += dppf<ROW_ROR8>(L); L = t16_add(L, L);
        stv = v4u{u_of(M) & 0xFFFF0000u, ep.x, u_of(L), ep.y};
      }
      asm volatile("" : "+v"(stv));
      bar();
      tb2 = now();
      if (wave == 3) { stv.x |= tb2 & 0xFFFFu; ST4(stv, rws, ast, 0); }
      FENCE();
    };

    if (roleB) {
      const int cgw = role * 2 + wave;
      const rsrc_t ro = mk_rsrc(wo + ((size_t)(kb * 512 + (rbk >> 1) * 128 + (rbk & 1) * 2) * D + cgw * 64) * 2, 125 * D * 2 + 128);
      int wooff = ((n >> 3) * 64 + g * 16) * (D * 2) + (n & 7) * 16;
      int a2w = WS_H2 + sd(((g & 1) * 32 + cgw * 4 + (n >> 2)) * 4096, pm >> (g * 2 + (cgw >> 2))) + (kb * 8 + rbk) * 64 + (n & 3) * 16;
      int lor = LDS0 + L_O + (n >> 2) * 128 + (n & 1) * 64 + g * 16;
      asm volatile("" : "+v"(a2w), "+v"(lor), "+v"(wooff));
      v4u wr[8], f[8];
      bar();
#pragma unroll
      for (int r = 0; r < 8; ++r) wr[r] = LD4(ro, wooff + (r & 1) * 1024, (r >> 1) * 4096, AUX_NT);
      to_b2();
      tr8(wr, f);
      pin(f);
      STAMP(7);
#define ADDL(r, d0, d1) "v_add_f32_dpp " r ", " d1 ", " d0 " row_ror:8 row_mask:0xf bank_mask:0x3\n\t"
#define ADDH(r, d0, d1) "v_add_f32_dpp " r ", " d0 ", " d1 " row_ror:8 row_mask:0xf bank_mask:0xc\n\t"
      asm volatile(
          "s_barrier\n\tds_read_b128 v[232:235], %[lo]\n\ts_waitcnt lgkmcnt(0)\n\t"
          MF("200:203", "f0") "s_nop 1\n\t" MF("204:207", "f1") "s_nop 1\n\t" MF("208:211", "f2") "s_nop 1\n\t" MF("212:215", "f3")
          ADDL("v236", "v200", "v201") "s_nop 0\n\t" MF("216:219", "f4") ADDL("v237", "v204", "v205") "s_nop 0\n\t" MF("220:223", "f5")
          ADDL("v238", "v208", "v209") "s_nop 0\n\t" MF("224:227", "f6") ADDL("v239", "v212", "v213") "s_nop 0\n\t" MF("228:231", "f7")
          ADDH("v236", "v216", "v217") "v_and_or_b32 v236, v236, %[m], %[t0]\n\ts_nop 0\n\t"
          ADDH("v237", "v220", "v221") "v_and_or_b32 v237, v237, %[m], %[t1]\n\t"
          ADDH("v238", "v224", "v225") "v_and_or_b32 v238, v238, %[m], %[t2]\n\ts_nop 0\n\t"
          ADDH("v239", "v228", "v229") "v_and_or_b32 v239, v239, %[m], %[t3]\n\t"
          "global_store_dwordx4 %[a], v[236:239], %[b] sc1\n\ts_nop 1"
          :: [lo] "v"(lor), [a] "v"(a2w), [b] "s"(ws), TAGS, FRAGS(f) : "memory", VTMP);
      STAMP(11);
    } else if (role == 1) {
      const int rid = col * 16 + rbk * 2 + (wave & 1);
      const int ocol = (rid >> 5) * D + ((rid >> 2) & 7) * 64 + ((rid & 1) * 4 + (lane & 3)) * 8 + ((rid >> 1) & 1) * 4 + g;
      float xf = bf_lo(*(const unsigned short*)(x + ocol * 2));
      const rsrc_t rout = mk_rsrc(out, 4 * D * 2), rp = mk_rsrc(w1, D * FF * 2);
      int a2 = WS_H2 + sd((rid & 63) * 4096, side) + lane * 16, a4 = a2 + (WS_H4 - WS_H2);
      int a3w = n < 8 ? WS_H3 + (n >> 2) * 8192 + (rid & 31) * 256 + (rid >> 5) * 64 + g * 16 + (n & 3) * 4 : OOB, aout = n < 4 ? ocol * 2 : OOB;
      int apf = (rbk * 64 + wave * 16 + (lane >> 2)) * (FF * 2) + ((col * 448) & ~127) + (lane & 3) * 128;
      u32 t3 = (lane & 2) ? ((lane & 1) ? T3 : T2) : ((lane & 1) ? T1 : T0);
      asm volatile("" : "+v"(a2), "+v"(a4), "+v"(a3w), "+v"(aout), "+v"(t3), "+v"(xf), "+v"(apf));
      bar();
      const u32 pfv = __builtin_amdgcn_raw_buffer_load_b32(rp, apf, 0, 0);
      to_b2();
      asm volatile("" :: "v"(pfv));
      if (wave < 2) {
        wait_until(tb2 + T_HINT_R);
        const u32 th = poll_times(ws, ahn, ep.x, 0xFFu, tb2);
        STAMP(9);
        wait_until(th + T_H2);
        u32 left;
        const u32 h = reduce4<1024>(ws, a2, T0, T1, T2, T3, 0xFFFFFFFFu, xf, left) << 16;
        __builtin_amdgcn_raw_buffer_store_b32(h | t3, rws, a3w, 0, AUX_SC1);
        FENCE();
        RETRY(10, left != SPIN_CAP - 1);
        STAMP(4);
        if (wg == 8 && tid == 0) ST2(ep, rws, 0, WS_E);
        wait_until(th + T_H4);
        const u32 y = reduce4<896>(ws, a4, T0, T1, T2, T3, 0x00FFFFFFu, f_of(h), left);
        __builtin_amdgcn_raw_buffer_store_b16((unsigned short)y, rout, aout, 0, 0);
        FENCE();
        RETRY(5, left != SPIN_CAP - 1);
      }
    } else if (roleF) {
      const rsrc_t r2 = mk_rsrc(w2 + ((size_t)(ug * 32) * D + wave * 128) * 2, 31 * D * 2 + 256);
      int w2off = g * (D * 2) + n * 16;
      int a4w = WS_H4 + sd(((g & 1) * 32 + (wave * 2 + (n >> 3)) * 4 + ((n >> 2) & 1)) * 4096, pm >> (g * 2 + (wave >> 1))) + ug * 64 + (n & 3) * 16, a4x = a4w + 16384;
      int lrc = LDS0 + L_RC + ((n >> 2) * 16 + g * 4 + (n & 3)) * 8, lln = LDS0 + L_LN + (n >> 2) * 32, lcs = LDS0 + L_CS + (g * 4 + (n & 3)) * 8;
      asm volatile("" : "+v"(lcs), "+v"(w2off), "+v"(a4w), "+v"(a4x), "+v"(lrc), "+v"(lln));
      v4u wb[8], f[8];
      bar();
      to_b2();
      wait_until(tb2 + T_W2);
#pragma unroll
      for (int r = 0; r < 8; ++r) wb[r] = LD4(r2, w2off, ((r >> 1) * 8 + (r & 1) * 4) * (D * 2), AUX_NT);
      FENCE();
      if (wave == 0) {
        wait_until(tb2 + T_HINT_F);
        const u32 th = poll_times(ws, ahn, ep.x, 0xFFu, tb2);
        STAMP(12);
        wait_until(th + T_H3);
      }
      bar();
      tr8(wb, f);
      pin(f);
      asm volatile(
          "s_barrier\n\t"
          "ds_read_b128 v[200:203], %[ln]\n\tds_read_b128 v[204:207], %[ln] offset:16\n\t"
          "ds_read2_b64 v[216:219], %[cs] offset1:16\n\tds_read2_b64 v[220:223], %[cs] offset0:32 offset1:48\n\t"
          "s_waitcnt lgkmcnt(3)\n\tv_add_f32 v200, v200, v202\n\tv_add_f32 v201, v201, v203\n\t"
          "s_waitcnt lgkmcnt(2)\n\tv_add_f32 v204, v204, v206\n\tv_add_f32 v205, v205, v207\n\t"
          "v_add_f32 v200, v200, v204\n\tv_add_f32 v201, v201, v205\n\t"
          "v_mul_f32 v200, %[k], v200\n\tv_mul_f32 v201, %[k], v201\n\tv_fma_f32 v201, -v200, v200, v201\n\tv_max_f32 v201, 0, v201\n\t"
          "v_cvt_pk_bf16_f32 v200, v201, v200\n\tv_lshlrev_b32 v201, 16, v200\n\tv_add_f32 v201, %[eps], v201\n\t"
          "s_barrier\n\t"
          "ds_read2st64_b64 v[208:211], %[rc] offset1:1\n\tds_read2st64_b64 v[212:215], %[rc] offset0:2 offset1:3\n\t"
          "v_cvt_pk_bf16_f32 v201, v201, v201\n\tv_lshlrev_b32 v201, 16, v201\n\tv_sqrt_f32 v201, v201\n\t"
          "v_and_b32 v200, %[hi], v200\n\tv_cvt_pk_bf16_f32 v201, v201, v201\n\tv_lshlrev_b32 v201, 16, v201\n\tv_rcp_f32 v201, v201\n\t"
          "s_waitcnt lgkmcnt(2)\n\tv_add_f32 v216, v216, v218\n\tv_add_f32 v217, v217, v219\n\tv_add_f32 v220, v220, v222\n\tv_add_f32 v221, v221, v223\n\t"
          "v_add_f32 v216, v216, v220\n\tv_add_f32 v217, v217, v221\n\t"
          "s_waitcnt lgkmcnt(1)\n\tv_add_f32 v208, v208, v210\n\tv_add_f32 v209, v209, v211\n\t"
          "s_waitcnt lgkmcnt(0)\n\tv_add_f32 v212, v212, v214\n\tv_add_f32 v213, v213, v215\n\t"
          "v_add_f32 v208, v208, v212\n\tv_add_f32 v209, v209, v213\n\t"
          "v_fma_f32 v208, -v200, v216, v208\n\tv_fma_f32 v209, -v200, v217, v209\n\tv_mul_f32 v208, v208, v201\n\tv_mul_f32 v209, v209, v201\n\t"
          "v_cvt_pk_bf16_f32 v209, v208, v209\n\tv_lshlrev_b32 v208, 16, v209\n\tv_and_b32 v209, %[hi], v209\n\t"
          "v_mul_f32 v210, %[nl], v208\n\tv_mul_f32 v211, %[nl], v209\n\tv_exp_f32 v210, v210\n\tv_exp_f32 v211, v211\n\t"
          "v_add_f32 v210, 1.0, v210\n\tv_add_f32 v211, 1.0, v211\n\tv_rcp_f32 v210, v210\n\tv_rcp_f32 v211, v211\n\t"
          "v_mul_f32 v208, v208, v210\n\tv_mul_f32 v209, v209, v211\n\tv_cvt_pk_bf16_f32 v232, v208, v209\n\ts_nop 1\n\t"
          "v_mov_b32_dpp v233, v232 row_shl:1" DPPM "v_mov_b32_dpp v234, v232 row_shl:2" DPPM "v_mov_b32_dpp v235, v232 row_shl:3" DPPM "s_nop 1\n\t"
          MF("200:203", "f0") "s_nop 1\n\t" MF("204:207", "f1") "s_nop 1\n\t" MF("208:211", "f2") "s_nop 1\n\t" MF("212:215", "f3")
          "v_and_or_b32 v236, v200, %[m], %[t0]\n\ts_nop 0\n\t" MF("216:219", "f4")
          "v_and_or_b32 v237, v204, %[m], %[t1]\n\ts_nop 0\n\t" MF("220:223", "f5")
          "v_and_or_b32 v238, v208, %[m], %[t2]\n\ts_nop 0\n\t" MF("224:227", "f6")
          "v_and_or_b32 v239, v212, %[m], %[t3]\n\tglobal_store_dwordx4 %[a0], v[236:239], %[b] sc1\n\t" MF("228:231", "f7")
          "v_and_or_b32 v240, v216, %[m], %[t0]\n\ts_nop 1\n\tv_and_or_b32 v241, v220, %[m], %[t1]\n\ts_nop 1\n\t"
          "v_and_or_b32 v242, v224, %[m], %[t2]\n\ts_nop 0\n\tv_and_or_b32 v243, v228, %[m], %[t3]\n\t"
          "global_store_dwordx4 %[a1], v[240:243], %[b] sc1\n\ts_nop 1"
          :: [ln] "v"(lln), [rc] "v"(lrc), [cs] "v"(lcs), [a0] "v"(a4w), [a1] "v"(a4x), [b] "s"(ws), TAGS,
             [k] "s"(1.f / D), [eps] "s"(1e-5f), [hi] "s"(0xFFFF0000u), [nl] "s"(-LOG2E), FRAGS(f)
          : "memory", VTMP);
      STAMP(13);
    } else {
      int asr = WS_ST + sd(((kb & 3) * 32 + (lane & 31)) * 128, pm >> kb);
      int ahw = WS_HN + sd((((lane >> 3) & 3) * 8 + kb) * 256, pm >> (lane >> 3)) + (lane & 7) * 16;
      asm volatile("" : "+v"(asr), "+v"(ahw));
      bar();
      to_b2();
      if (wave == 0) {
        const u32 tm = poll_times(ws, asr, ep.x, 0xFFFFFFFFu, tb2);
        ST4((v4u{tm, ep.x, 0u, 0u}), rws, ahw, 0);
        STAMP(8);
      }
    }
  } else {
    const int w4 = wave - 4;
    __builtin_amdgcn_s_setprio(3);
    int xso = (n >> 2) * 1024 + ((n & 3) + g * 4) * 16, xao = (n >> 2) * 1024 + ((n & 1) * 64 + g * 8) * 2, wqo = ((n >> 3) * 64 + g * 8) * (QD * 2) + (n & 7) * 16;
    u32 wqb = (u32)(role * 128 * QD + col * 512 + rbk * 64) * 2;
    asm volatile("" : "+v"(xso), "+v"(xao), "+v"(wqo));
    asm volatile("" : "+s"(wqb));
    FENCE();
    ARGS();
    v2u eold;
    u32 tst;
    if (__builtin_expect(wave == 7, 1)) {
      const rsrc_t rx = mk_rsrc(x, 4 * D * 2), rq = mk_rsrc(wq + wqb, 127 * QD * 2 + 128);
      v4u xs[4], xa[2], wr[2][8];
#pragma unroll
      for (int i = 0; i < 4; ++i) xs[i] = LD4(rx, xso + i * 256, 0, 0);
#pragma unroll
      for (int u = 0; u < 2; ++u) xa[u] = LD4(rx, xao + u * 64, role * 256, 0);
#pragma unroll
      for (int u = 0; u < 2; ++u)
#pragma unroll
        for (int r = 0; r < 8; ++r) wr[u][r] = LD4(rq, wqo, (u * 32 + r) * (QD * 2), AUX_NT);
      eold = LD2(rws, e_off, WS_E);
      FENCE();
      tst = now() - 20u;
      asm volatile("s_barrier" ::: "memory");
      FENCE();
      float sum = 0.f;
#pragma unroll
      for (int i = 0; i < 4; ++i)
#pragma unroll
        for (int r = 0; r < 4; ++r) sum += bf_lo(xs[i][r]) + bf_hi(xs[i][r]);
      const float mean = quad_rows_sum(sum) * (1.f / D);
      float sq = 0.f;
#pragma unroll
      for (int i = 0; i < 4; ++i)
#pragma unroll
        for (int r = 0; r < 4; ++r) { const float a = bf_lo(xs[i][r]) - mean, c = bf_hi(xs[i][r]) - mean; sq += a * a + c * c; }
      const float var = quad_rows_sum(sq) * (1.f / D);
      const float mb = rbf(mean), rs = __builtin_amdgcn_rcpf(rbf(__builtin_amdgcn_sqrtf(rbf(rbf(var) + 1e-5f))));
      v4f qa[8];
#pragma unroll
      for (int t = 0; t < 8; ++t) qa[t] = ZERO4;
#pragma unroll
      for (int u = 0; u < 2; ++u) {
        v4u an, f[8];
#pragma unroll
        for (int r = 0; r < 4; ++r) { const u32 c = pk_bf16(bf_lo(xa[u][r]) - mb, bf_hi(xa[u][r]) - mb); an[r] = pk_bf16(bf_lo(c) * rs, bf_hi(c) * rs); }
        tr8(wr[u], f);
#pragma unroll
        for (int t = 0; t < 8; ++t) qa[t] = mfma32(an, f[t], qa[t]);
      }
      const v2u e = next_epoch(eold);
      const u32 e0 = (n & 1) ? e.y : e.x;
      v4u qo;
#pragma unroll
      for (int i = 0; i < 4; ++i) {
        const float lo = qa[i][0] + dppf<ROW_ROR8>(qa[i][1]), hi = qa[4 + i][1] + dppf<ROW_ROR8>(qa[4 + i][0]);
        qo[i] = ((u_of(n < 8 ? lo : hi) + 8u) & ~15u) | ((e0 >> (8 * i)) & 15u);
      }
      ST4(qo, rws, g * 8192 + n * 16, WS_QP + (col * 16 + role) * 2048 + rbk * 256);
      STAMP(3);
    } else {
      tst = now();
      asm volatile("s_barrier" ::: "memory");
      v2u es;
      asm volatile("s_load_dwordx2 %0, %1, 0x0\n\ts_waitcnt lgkmcnt(0)" : "=&s"(es) : "s"(ws) : "memory");
      if (j == 0 && wave == 4) ST4((v4u{tst, next_epoch(es).y, 0u, 0u}), rws, lane < 8 ? lane * 16 : OOB, WS_OR + col * 256);
      eold = LD2(rws, e_off, WS_E);
      FENCE();
    }
    __builtin_amdgcn_s_setprio(0);
    u32 vote = 0;
    if (j < 8 && (wave == 5 || wave == 6)) {
      const char* pa = ws + WS_PR + (wg >> 4) * 16384 + (wave - 5) * (8192 + 256) + (wg & 15) * 512;
      const char* pb = (const char*)((u64)pa ^ 8192ull);
      u64 c0, c1, c2;
      u32 sink;
      asm volatile("s_memtime %0\n\ts_load_dword %3, %4, 0x0\n\ts_waitcnt lgkmcnt(0)\n\ts_memtime %1\n\ts_load_dword %3, %5, 0x0\n\ts_waitcnt lgkmcnt(0)\n\t"
                   "s_memtime %2\n\ts_waitcnt lgkmcnt(0)"
                   : "=&s"(c0), "=&s"(c1), "=&s"(c2), "=&s"(sink) : "s"(pa), "s"(pb) : "memory");
      const u32 bnear = (u32)c1 - (u32)c0 > (u32)c2 - (u32)c1 ? 1u : 0u;
      const u32 die = ((u32)__builtin_amdgcn_s_getreg(20 | (3 << 11)) >> 2) & 1u;
      vote = die * 2u | ((bnear ^ (u32)(wave - 5) ^ die) & 1u);
    }
    asm volatile("" : "+v"(eold) : "v"(code_pf));
    const v2u ep = next_epoch(eold);
    if (j < 8 && (wave == 5 || wave == 6)) ST2((v2u{(ep.x & ~3u) | vote, ep.y}), rws, e_off, WS_VT + wg * 16 + (wave - 5) * 8);
    MAKE_TAGS();
    int asr = WS_ST + (w4 * 8 + (lane & 7)) * 128 + (rbk >> 1) * 16;
    int a1w = WS_H1 + sd(((grp & 15) * 8 + s) * 2048 + w4 * 512 + lane * 8, 0);
    int a1 = WS_H1 + sd((w4 * 8 + g) * 2048 + rbk * 256 + n * 16, 0), a1b;
    int lrd = LDS0 + L_ACC + half * 8192 + w4 * 512 + (lane & 31) * 16, lfw = LDS0 + L_FW + w4 * 32 + half * 16;
    int lwr = L_ACC + wave * 2048 + lane * 32, low = LDS0 + L_O + w4 * 128 + n * 8 + g * 2, g4 = g * 4;
    u32 Ta = (lane & 1) ? T2 : T0, Tb = (lane & 1) ? T3 : T1;
    asm volatile("" : "+v"(lfw), "+v"(a1w), "+v"(lrd), "+v"(lwr), "+v"(Ta), "+v"(Tb), "+v"(a1), "+v"(low), "+v"(g4), "+v"(asr), "+v"(pm0), "+v"(pm1));
    if (wave == 4) {
#pragma unroll 1
      for (int i = 0; i < 64 && (int)(now() - tst - T_ORIGIN) < 0; ++i) __builtin_amdgcn_s_sleep(4);
      const v2u og = LD2(rws, lane < 8 ? lane * 256 : OOB, WS_OR);
      int dt = og.y == ep.y ? min(max((int)(og.x - tst), -200), 200) : (int)T_ORIGIN - 120;
      dt += (int)dppu<QP_X1>((u32)dt); dt += (int)dppu<QP_X2>((u32)dt); dt += (int)dppu<ROW_HMIRROR>((u32)dt);
      if (lane == 0) *(lds_vu32*)(lds + L_FLAG + 36) = (u32)(dt >> 3);
    }
    due += tst + 8u * PACE_T;
    STREAM_PREP();
    KV_RSRC();
    lvl = lds_wait(lds + L_FLAG + w4 * 4) & 7u;
    target = table + lvl * 8u;
    int pm = 0;
    int ash;
    u32 P[4];
    {
      v4u qb;
      v4f d[16];
      float t[16], S[4];
      unroll<32>([&](auto ic) {
        constexpr int i = decltype(ic)::value;
        STREAM(i, true);
        if (i == 6) due += uni(*(lds_vu32*)(lds + L_FLAG + 36));
        if (i == QK_AT - 1) { lds_wait(lds + L_FLAG + 16 + (lane & 3) * 4); qb = *(const v4u*)(lds + lq); }
        unroll<16>([&](auto kc_) {
          constexpr int k = decltype(kc_)::value;
          if constexpr (k >= qk_done(i - 2) && k < qk_done(i - 1)) {
            t[k] = quad_sum<k & 3>(d[k]);
            asm volatile("" : "+v"(t[k]));
            if constexpr ((k & 3) == 3) { S[k >> 2] = score4(t[k - 3], t[k - 2], t[k - 1], t[k], lane); asm volatile("" : "+v"(S[k >> 2])); }
          }
          if constexpr (k >= qk_done(i - 1) && k < qk_done(i)) d[k] = mfma32(K[k], qb, ZERO4);
        });
        if (i == W_MAX + 17) { softmax64(S, lds, wave, lane, P); asm volatile("" : "+v"(P[0]), "+v"(P[1]), "+v"(P[2]), "+v"(P[3])); }
        if (i == PM_AT) {
          pm = (int)lds_wait(lds + L_FLAG + 32) & 255;
          const int kb = partner(col, pm);
          asr += sd((kb & 3) * 4096, pm >> kb); a1w += ((pm >> col) & 1) << 13; a1 += sd((kb & 3) * 65536, pm >> kb); a1b = a1 + 16384;
          ash = (roleB && lane < 8) ? asr : OOB;
          asm volatile("" : "+v"(asr), "+v"(a1w), "+v"(a1), "+v"(a1b), "+v"(ash));
        }
        FENCE();
      });
    }
    v4u stt = LD4(rws, ash, 0, AUX_SC1);
    FENCE();
    STAMP(6);
    bar();
    pv_store(P, pm0, pm1, V, lds + lwr);
    asm volatile("" : "+v"(stt));
#define B2ASM "s_waitcnt lgkmcnt(0)\n\ts_barrier\n\t"
    FENCE();
    asm volatile(B2ASM
        "ds_read_b128 v[200:203], %[lf]\n\tds_read_b128 v[204:207], %[lr]\n\tds_read_b128 v[208:211], %[lr] offset:2048\n\t"
        "ds_read_b128 v[212:215], %[lr] offset:4096\n\tds_read_b128 v[216:219], %[lr] offset:6144\n\t"
        "s_waitcnt lgkmcnt(3)\n\tv_mul_f32 v204, v200, v204\n\tv_mul_f32 v205, v200, v205\n\tv_mul_f32 v206, v200, v206\n\tv_mul_f32 v207, v200, v207\n\t"
        "s_waitcnt lgkmcnt(2)\n\tv_fmac_f32 v204, v201, v208\n\tv_fmac_f32 v205, v201, v209\n\tv_fmac_f32 v206, v201, v210\n\tv_fmac_f32 v207, v201, v211\n\t"
        "s_waitcnt lgkmcnt(1)\n\tv_fmac_f32 v204, v202, v212\n\tv_fmac_f32 v205, v202, v213\n\tv_fmac_f32 v206, v202, v214\n\tv_fmac_f32 v207, v202, v215\n\t"
        "s_waitcnt lgkmcnt(0)\n\tv_fmac_f32 v204, v203, v216\n\tv_fmac_f32 v206, v203, v218\n\tv_fmac_f32 v205, v203, v217\n\tv_fmac_f32 v207, v203, v219\n\t"
        "v_permlane32_swap_b32 v204, v206\n\tv_add_f32 v204, v204, v206\n\tv_permlane32_swap_b32 v205, v207\n\tv_add_f32 v205, v205, v207\n\t"
        "v_and_or_b32 v204, v204, %[m], %[ta]\n\tv_and_or_b32 v205, v205, %[m], %[tb]\n\t"
        "global_store_dwordx2 %[a], v[204:205], %[b] sc1"
        :: [lf] "v"(lfw), [lr] "v"(lrd), [a] "v"(a1w), [b] "s"(ws), [ta] "v"(Ta), [tb] "v"(Tb), [m] "s"(KEEP) : "memory", VTMP);
    if (roleB) {
      u32 cnt = SPIN_CAP;
      asm volatile(
          "s_mov_b64 exec, 0xff\n\tv_cmpx_ne_u32 vcc, %[ex], v221\n\ts_cbranch_execz 2f\n\ts_sleep %[gd]\n\t"
          "1:\n\tglobal_load_dwordx4 v[220:223], %[as], %[b] sc1\n\ts_sub_u32 %[c], %[c], 1\n\ts_cbranch_scc1 2f\n\t"
          "s_waitcnt vmcnt(0)\n\tv_cmpx_ne_u32 vcc, %[ex], v221\n\ts_cbranch_execnz 1b\n\t"
          "2:\n\ts_mov_b64 exec, -1\n\t"
          "global_load_dwordx4 v[200:203], %[a0], %[b] sc1\n\tglobal_load_dwordx4 v[204:207], %[a1], %[b] sc1\n\t"
          "v_and_b32 v224, 0xffff0000, v220\n\tv_cmp_ne_u32 vcc, %[ey], v223\n\ts_nop 0\n\t"
          "v_max_f32_dpp v227, v224, v224 quad_perm:[1,0,3,2]" DPPM "s_nop 1\n\t"
          "v_max_f32_dpp v227, v227, v227 quad_perm:[2,3,0,1]" DPPM "s_nop 1\n\t"
          "v_max_f32_dpp v227, v227, v227 row_half_mirror" DPPM
          "s_and_b32 s90, vcc_lo, 0xff\n\t"
          "v_sub_f32 v227, v224, v227\n\tv_mul_f32 v227, 0x3fb8aa3b, v227\n\tv_exp_f32 v227, v227\n\ts_nop 0\n\t"
          "v_mul_f32 v228, v227, v222\n\ts_nop 1\n\t"
          "v_add_f32_dpp v228, v228, v228 quad_perm:[1,0,3,2]" DPPM "s_nop 1\n\t"
          "v_add_f32_dpp v228, v228, v228 quad_perm:[2,3,0,1]" DPPM "s_nop 1\n\t"
          "v_add_f32_dpp v228, v228, v228 row_half_mirror" DPPM
          "v_rcp_f32 v228, v228\n\ts_nop 0\n\tv_mul_f32 v227, v227, v228\n\t"
          "ds_bpermute_b32 v228, %[g4], v227\n\tds_bpermute_b32 v229, %[g4], v227 offset:16\n\ts_waitcnt lgkmcnt(0)\n\t"
          "s_waitcnt vmcnt(1)\n\t"
          "v_xor_b32 v200, v200, %[t0]\n\tv_xor_b32 v201, v201, %[t1]\n\tv_xor_b32 v202, v202, %[t2]\n\tv_xor_b32 v203, v203, %[t3]\n\t"
          "v_mul_f32 v208, v228, v200\n\tv_mul_f32 v209, v228, v201\n\tv_mul_f32 v210, v228, v202\n\tv_mul_f32 v211, v228, v203\n\t"
          "s_waitcnt vmcnt(0)\n\t"
          "v_xor_b32 v204, v204, %[t0]\n\tv_xor_b32 v206, v206, %[t2]\n\tv_xor_b32 v205, v205, %[t1]\n\tv_xor_b32 v207, v207, %[t3]\n\t"
          "v_fmac_f32 v208, v229, v204\n\tv_fmac_f32 v210, v229, v206\n\tv_fmac_f32 v209, v229, v205\n\tv_fmac_f32 v211, v229, v207\n\t"
          "v_permlane32_swap_b32 v208, v210\n\tv_or3_b32 v212, v200, v201, v202\n\tv_permlane32_swap_b32 v209, v211\n\t"
          "v_add_f32 v208, v208, v210\n\tv_add_f32 v209, v209, v211\n\t"
          "v_or3_b32 v213, v203, v204, v205\n\tv_or3_b32 v212, v212, v206, v207\n\t"
          "v_permlane16_swap_b32 v208, v209\n\tv_add_f32 v208, v208, v209\n\tv_cvt_pk_bf16_f32 v208, v208, v208\n\t"
          "ds_write_b16 %[lo], v208\n\t"
          "v_bitop3_b32 v212, v212, v213, %[m] bitop3:0x54\n\tv_cmp_ne_u32 vcc, 0, v212\n\t"
          "s_or_b32 s90, s90, vcc_lo\n\ts_or_b32 s90, s90, vcc_hi\n\ts_cbranch_scc0 3f\n\t"
          "s_mov_b64 exec, 0xff\n\ts_cmp_lt_i32 %[c], 1\n\ts_cbranch_scc0 1b\n\ts_mov_b64 exec, -1\n\t"
          "3:\n\ts_waitcnt lgkmcnt(0)\n\ts_barrier"
          : [c] "+s"(cnt), "+{v[220:223]}"(stt)
          : [as] "v"(asr), [a0] "v"(a1), [a1] "v"(a1b), [b] "s"(ws), [ex] "s"(ep.x), [ey] "s"(ep.y), TAGS, [g4] "v"(g4), [lo] "v"(low), [gd] "i"(G_SLEEP)
          : "memory", "vcc", "scc", "s90", "v200", "v201", "v202", "v203", "v204", "v205", "v206", "v207", "v208", "v209", "v210", "v211", "v212", "v213",
            "v224", "v227", "v228", "v229");
    } else if (roleF) {
      const rsrc_t r1 = mk_rsrc(w1 + ((size_t)(w4 * 128) * FF + ug * 32) * 2, 127 * FF * 2 + 64);
      int w1off = ((g >> 1) * 64 + (g & 1) * 4 + (n >> 2)) * (FF * 2) + (n & 3) * 16;
      v4u wa[8], f[8];
      asm volatile("" : "+v"(w1off));
      FENCE();
      __builtin_amdgcn_s_sleep(W1_SLEEP);
      FENCE();
#pragma unroll
      for (int r = 0; r < 8; ++r) wa[r] = LD4(r1, w1off, r * 8 * (FF * 2), 0);
      FENCE();
      tr8(wa, f);
      const v4u ones = {0x3F803F80u, 0x3F803F80u, 0x3F803F80u, 0x3F803F80u};
      int a3 = WS_H3 + sd((w4 * 8 + g * 2) * 256, pm >> col) + n * 16;
      int lrc = LDS0 + L_RC + w4 * 512 + lane * 8, lln = LDS0 + (n == g * 4 ? L_LN + g * 32 + w4 * 8 : L_DUM + lane * 8), lcs = LDS0 + L_CS + w4 * 128 + n * 8;
      pin(f);
      asm volatile("" : "+v"(a3), "+v"(lrc), "+v"(lln), "+v"(lcs));
      STAMP(7);
      u32 cnt = SPIN_CAP;
#define M1(d, f) "v_mfma_f32_16x16x32_bf16 v[" d "], %[one], %[" f "], 0\n\t"
#define R12(r, x, y) "v_add_f32_dpp " r ", " y ", " x " row_ror:12" DPPM
#define R8M(r, a, c, bm) "v_add_f32_dpp " r ", " c ", " a " row_ror:8 row_mask:0xf bank_mask:" bm "\n\t"
      asm volatile(
          "s_waitcnt lgkmcnt(0)\n\ts_barrier\n\t"
          "1:\n\tglobal_load_dwordx4 v[244:247], %[a], %[b] sc1\n\tglobal_load_dwordx4 v[248:251], %[a], %[b] offset:256 sc1\n\ts_sub_u32 %[c], %[c], 1\n\t"
          M1("200:203", "f0") M1("204:207", "f1") M1("208:211", "f2") M1("212:215", "f3") M1("216:219", "f4") M1("220:223", "f5") M1("224:227", "f6") M1("228:231", "f7")
          "s_nop 0\n\t" R12("v200", "v200", "v200") R12("v204", "v204", "v204") R12("v208", "v208", "v208") R12("v212", "v212", "v212")
          R12("v216", "v216", "v216") R12("v220", "v220", "v220") R12("v224", "v224", "v224") R12("v228", "v228", "v228")
          R8M("v252", "v200", "v200", "0x1") R8M("v253", "v216", "v216", "0x1") "s_nop 0\n\t" R8M("v252", "v204", "v204", "0x2") R8M("v253", "v220", "v220", "0x2") "s_nop 0\n\t"
          R8M("v252", "v208", "v208", "0x4") R8M("v253", "v224", "v224", "0x4") "s_nop 0\n\t" R8M("v252", "v212", "v212", "0x8") R8M("v253", "v228", "v228", "0x8")
          "ds_write_b64 %[cs], v[252:253]\n\t"
          "s_waitcnt vmcnt(1)\n\tv_perm_b32 v232, v245, v244, %[sel]\n\tv_perm_b32 v233, v247, v246, %[sel]\n\t"
          "s_waitcnt vmcnt(0)\n\tv_perm_b32 v234, v249, v248, %[sel]\n\tv_perm_b32 v235, v251, v250, %[sel]\n\t"
          "v_bitop3_b32 v244, v244, v248, %[t0] bitop3:0x7e\n\tv_bitop3_b32 v245, v245, v249, %[t1] bitop3:0x7e\n\t"
          "v_mfma_f32_16x16x32_bf16 v[240:243], v[232:235], v[232:235], 0\n\t"
          "v_bitop3_b32 v246, v246, v250, %[t2] bitop3:0x7e\n\tv_bitop3_b32 v247, v247, v251, %[t3] bitop3:0x7e\n\t"
          MF("236:239", "one") "v_or3_b32 v244, v244, v245, v246\n\tv_bitop3_b32 v244, v244, v247, %[m] bitop3:0x54\n\t"
          MF("200:203", "f0") "v_cmp_ne_u32 vcc, 0, v244\n\ts_nop 0\n\t"
          MF("204:207", "f1") "v_add_f32_dpp v240, v241, v240 quad_perm:[1,1,1,1]" DPPM "v_add_f32_dpp v242, v243, v242 quad_perm:[3,3,3,3]" DPPM
          MF("208:211", "f2") "v_add_f32 v236, v236, v237\n\tv_add_f32 v238, v238, v239\n\t"
          MF("212:215", "f3") "v_add_f32_dpp v255, v242, v240 quad_perm:[2,2,2,2]" DPPM "v_add_f32 v254, v236, v238\n\t"
          "ds_write_b64 %[ln], v[254:255]\n\t"
          MF("216:219", "f4") R12("v244", "v200", "v201") R12("v245", "v202", "v203")
          "s_cbranch_scc1 2f\n\ts_cbranch_vccnz 1b\n\t2:\n\t"
          MF("220:223", "f5") R12("v246", "v205", "v206") R12("v247", "v207", "v204")
          MF("224:227", "f6") R12("v248", "v210", "v211") R12("v249", "v208", "v209")
          MF("228:231", "f7") "s_waitcnt lgkmcnt(0)\n\ts_barrier\n\t"
          R12("v250", "v215", "v212") R12("v251", "v213", "v214") R12("v200", "v216", "v217") R12("v201", "v218", "v219")
          R12("v202", "v221", "v222") R12("v203", "v223", "v220") R12("v204", "v226", "v227") R12("v205", "v224", "v225")
          R8M("v252", "v244", "v245", "0x1") R12("v206", "v231", "v228") R8M("v253", "v200", "v201", "0x1") R12("v207", "v229", "v230")
          R8M("v252", "v246", "v247", "0x2") R8M("v253", "v202", "v203", "0x2") "s_nop 0\n\t"
          R8M("v252", "v248", "v249", "0x4") R8M("v253", "v204", "v205", "0x4") "s_nop 0\n\t"
          R8M("v252", "v250", "v251", "0x8") R8M("v253", "v206", "v207", "0x8")
          "ds_write_b64 %[rc], v[252:253]\n\ts_waitcnt lgkmcnt(0)\n\ts_barrier"
          : [c] "+s"(cnt)
          : [a] "v"(a3), [b] "s"(ws), [sel] "s"(0x07060302u), TAGS, [rc] "v"(lrc), [ln] "v"(lln), [cs] "v"(lcs), [one] "v"(ones), FRAGS(f)
          : "memory", "vcc", "scc", VTMP);
      RETRY(14, cnt != SPIN_CAP - 1);
      STAMP(13);
    }
  }
}

extern "C" void kernel_launch(void* const* d_in, const int* in_sizes, int n_in, void* d_out, int out_size, void* d_ws, size_t ws_size,
                              hipStream_t stream) {
  fused_decode_kernel<<<NWG, 512, 0, stream>>>((const char*)d_in[1], (const char*)d_in[2], (char*)d_ws, (const char*)d_in[0], (const char*)d_in[3],
                                               (const char*)d_in[4], (const char*)d_in[6], (const char*)d_in[7], (char*)d_out
  );
}
```
